```python
import torch

def _softmax(x, axis=-1):
    return torch.softmax(x, dim=axis)
def _gelu(x):
    return torch.nn.functional.gelu(x, approximate="tanh")
def _silu(x):
    return torch.nn.functional.silu(x)
def _sigmoid(x):
    return torch.sigmoid(x)
def _layernorm(x, axis=-1, eps=1e-5):
    m = x.mean(dim=axis, keepdim=True)
    v = x.var(dim=axis, keepdim=True, unbiased=False)
    return (x - m) / torch.sqrt(v + eps)
def _scatter_add(tgt, idx, src, axis=0):
    return tgt.clone().index_add_(axis, idx, src)
def _segment_sum(src, seg, n):
    out = torch.zeros((n,) + src.shape[1:], dtype=src.dtype)
    return out.index_add_(0, seg, src)
def _pad(x, pw):
    flat = tuple(v for pair in reversed(pw) for v in pair)
    return torch.nn.functional.pad(x, flat)

kernel_name = 'sequential_preLN__spectral_fnet__dense_activation_squared_relu__prompt'

def setup_inputs():

    _w = (lambda a: (lambda t: t.to(torch.bfloat16) if t.is_floating_point() else t)(torch.as_tensor(a)))
    def _A_setup():
        B, S, D = 1, 2048, 640
        ns = torch.arange(S)[:, None] * torch.arange(S)[None, :]
        nd = torch.arange(D)[:, None] * torch.arange(D)[None, :]
        Ws_re = torch.cos(-2.0 * torch.pi * ns / S).to(float)
        Ws_im = torch.sin(-2.0 * torch.pi * ns / S).to(float)
        Wd_re = torch.cos(-2.0 * torch.pi * nd / D).to(float)
        Wd_im = torch.sin(-2.0 * torch.pi * nd / D).to(float)
        return {
            "x": _w((lambda *s: torch.randn(*s, dtype=torch.bfloat16) * 0.05)(B, S, D)),
            "dft_seq_re": _w(Ws_re),
            "dft_seq_im": _w(Ws_im),
            "dft_hid_re": _w(Wd_re),
            "dft_hid_im": _w(Wd_im),
        }
    _A_in = _A_setup()

    def _B_setup():
        B, S, D, F = 1, 2048, 640, 1728
        return {
            "x": _w((lambda *s: torch.randn(*s, dtype=torch.bfloat16) * 0.05)(B, S, D)),
            "w1": _w((lambda *s: torch.randn(*s, dtype=torch.bfloat16) * 0.2)(D, F)),
            "b1": _w((lambda *s: torch.randn(*s, dtype=torch.bfloat16) * 0.2)(F)),
            "w2": _w((lambda *s: torch.randn(*s, dtype=torch.bfloat16) * 0.2)(F, D)),
            "b2": _w((lambda *s: torch.randn(*s, dtype=torch.bfloat16) * 0.2)(D)),
        }
    _B_in = _B_setup()

    return {
        "A_x": _A_in["x"],
        "A_dft_seq_re": _A_in["dft_seq_re"],
        "A_dft_seq_im": _A_in["dft_seq_im"],
        "A_dft_hid_re": _A_in["dft_hid_re"],
        "A_dft_hid_im": _A_in["dft_hid_im"],
        "B_x": _B_in["x"],
        "B_w1": _B_in["w1"],
        "B_b1": _B_in["b1"],
        "B_w2": _B_in["w2"],
        "B_b2": _B_in["b2"],
    }

def reference(A_x, A_dft_seq_re, A_dft_seq_im, A_dft_hid_re, A_dft_hid_im, B_x, B_w1, B_b1, B_w2, B_b2):
    def _A_ref(x, dft_seq_re, dft_seq_im, dft_hid_re, dft_hid_im):
        a = torch.einsum("bsd,ed->bse", x, dft_hid_re)
        b = torch.einsum("bsd,ed->bse", x, dft_hid_im)
        y = torch.einsum("ts,bse->bte", dft_seq_re, a) - torch.einsum("ts,bse->bte", dft_seq_im, b)
        return y

    def _B_ref(x, w1, b1, w2, b2):
        h = torch.einsum("bsd,df->bsf", x, w1) + b1
        zero = torch.zeros_like(h)
        r = torch.where(h > zero, h, zero)
        h = r * r
        out = torch.einsum("bsf,fd->bsd", h, w2) + b2
        return out

    x = A_x
    h = x + _A_ref(_layernorm(x, axis=-1), A_dft_seq_re, A_dft_seq_im, A_dft_hid_re, A_dft_hid_im)
    h2 = h + _B_ref(_layernorm(h, axis=-1), B_w1, B_b1, B_w2, B_b2)
    return h2
```

```cpp
#include <hip/hip_runtime.h>
#include <hip/hip_bf16.h>
typedef __hip_bfloat16 bf16;
typedef __attribute__((ext_vector_type(8))) short bf16x8;
typedef __attribute__((ext_vector_type(4))) float f32x4;
static __device__ __forceinline__ float b2f(unsigned short u){ return __uint_as_float(((unsigned)u)<<16); }
static __device__ __forceinline__ unsigned short f2b(float f){ __hip_bfloat16 b = __float2bfloat16(f); return *(unsigned short*)&b; }
constexpr int S=2048,D=640,F=1728;
__global__ void ln_kernel(const unsigned short* x, unsigned short* y){
  int row=blockIdx.x; int t=threadIdx.x;
  float v[10]; float s=0;
  for(int i=0;i<10;i++){ v[i]=b2f(x[row*D+t+64*i]); s+=v[i]; }
  for(int o=32;o>0;o>>=1) s+=__shfl_xor(s,o);
  float m=s/D; float q=0;
  for(int i=0;i<10;i++){ float d=v[i]-m; q+=d*d; }
  for(int o=32;o>0;o>>=1) q+=__shfl_xor(q,o);
  float r=rsqrtf(q/D+1e-5f);
  for(int i=0;i<10;i++) y[row*D+t+64*i]=f2b((v[i]-m)*r);
}
template<bool BT,int MODE>
__global__ void gemm(const unsigned short* A,int lda,const unsigned short* B,int ldb,const unsigned short* A2,const unsigned short* B2,
   unsigned short* C,int ldc,int K,const unsigned short* bias,const unsigned short* res){
  int lane=threadIdx.x; int m0=blockIdx.y*16,n0=blockIdx.x*16;
  f32x4 acc={0,0,0,0}, acc2={0,0,0,0};
  for(int k=0;k<K;k+=32){
    bf16x8 a=*(const bf16x8*)(A+(size_t)(m0+(lane&15))*lda+k+8*(lane>>4));
    bf16x8 b;
    if(BT) b=*(const bf16x8*)(B+(size_t)(n0+(lane&15))*ldb+k+8*(lane>>4));
    else for(int j=0;j<8;j++) b[j]=B[(size_t)(k+8*(lane>>4)+j)*ldb+n0+(lane&15)];
    acc=__builtin_amdgcn_mfma_f32_16x16x32_bf16(a,b,acc,0,0,0);
    if(MODE==1){
      bf16x8 a2=*(const bf16x8*)(A2+(size_t)(m0+(lane&15))*lda+k+8*(lane>>4));
      bf16x8 b2=*(const bf16x8*)(B2+(size_t)(n0+(lane&15))*ldb+k+8*(lane>>4));
      acc2=__builtin_amdgcn_mfma_f32_16x16x32_bf16(a2,b2,acc2,0,0,0);
    }
  }
  for(int j=0;j<4;j++){
    int m=m0+(lane>>4)*4+j, n=n0+(lane&15);
    float v=acc[j];
    if(MODE==1) v=acc[j]-acc2[j]+b2f(res[m*ldc+n]);
    if(MODE==2){ v+=b2f(bias[n]); v=v>0?v*v:0; }
    if(MODE==3){ v+=b2f(bias[n])+b2f(res[m*ldc+n]); }
    C[(size_t)m*ldc+n]=f2b(v);
  }
}
extern "C" void kernel_launch(void* const* d_in, const int* in_sizes, int n_in, void* d_out, int out_size, void* d_ws, size_t ws_size, hipStream_t stream){
  typedef const unsigned short* cp;
  cp x=(cp)d_in[0], Cr=(cp)d_in[1], Ci=(cp)d_in[2], Dr=(cp)d_in[3], Di=(cp)d_in[4], w1=(cp)d_in[6], b1=(cp)d_in[7], w2=(cp)d_in[8], b2=(cp)d_in[9];
  unsigned short* ws=(unsigned short*)d_ws;
  unsigned short* xn=ws; unsigned short* aT=xn+S*D; unsigned short* bT=aT+S*D; unsigned short* h=bT+S*D; unsigned short* hn=h+S*D; unsigned short* f=hn+S*D;
  ln_kernel<<<S,64,0,stream>>>(x,xn);
  gemm<true,0><<<dim3(S/16,D/16),64,0,stream>>>(Dr,D,xn,D,nullptr,nullptr,aT,S,D,nullptr,nullptr);
  gemm<true,0><<<dim3(S/16,D/16),64,0,stream>>>(Di,D,xn,D,nullptr,nullptr,bT,S,D,nullptr,nullptr);
  gemm<true,1><<<dim3(D/16,S/16),64,0,stream>>>(Cr,S,aT,S,Ci,bT,h,D,S,nullptr,x);
  ln_kernel<<<S,64,0,stream>>>(h,hn);
  gemm<false,2><<<dim3(F/16,S/16),64,0,stream>>>(hn,D,w1,F,nullptr,nullptr,f,F,D,b1,nullptr);
  gemm<false,3><<<dim3(D/16,S/16),64,0,stream>>>(f,F,w2,D,nullptr,nullptr,(unsigned short*)d_out,D,F,b2,h);
}
```

```cpp
#include <hip/hip_runtime.h>
#include <hip/hip_bf16.h>

typedef unsigned short u16;
typedef unsigned int u32;
typedef unsigned long long u64;
typedef __attribute__((ext_vector_type(4))) u32 u32x4;
typedef __attribute__((ext_vector_type(2))) u32 u32x2;
typedef __attribute__((ext_vector_type(4))) float f32x4;
typedef __attribute__((ext_vector_type(2))) float f32x2;
typedef __attribute__((ext_vector_type(8))) __bf16 bf16x8;
typedef __attribute__((ext_vector_type(2))) __bf16 bf16x2;
typedef __attribute__((ext_vector_type(4))) short s16x4;
typedef __amdgpu_buffer_rsrc_t rsrc_t;
#define LAS __attribute__((address_space(3)))
#define DEV static __device__ __forceinline__

constexpr int S = 2048, D = 640, F = 1728;
constexpr u32 O_XNT = 0;
constexpr u32 O_W1P = O_XNT + 2621440;
constexpr u32 O_W2P = O_W1P + 2211840;
constexpr u32 O_DP  = O_W2P + 2211840;
constexpr u32 O_PQ  = O_DP + 1638400;
constexpr u32 O_H   = O_PQ + 5242880;
constexpr u32 O_F   = O_H + 2621440;
constexpr u32 O_CS  = O_F + 7077888;
constexpr u32 O_ST  = O_CS + 27648;
constexpr u32 WS_END = O_ST + 131072;
constexpr int LDS_BYTES = 159744;

struct Params {
  const void *x, *cr, *ci, *dr, *di, *w1, *b1, *w2, *b2;
  void *out, *ws;
};

DEV rsrc_t mkrs(const void* p, u32 bytes) { return __builtin_amdgcn_make_buffer_rsrc((void*)p, 0, (int)bytes, 0x00020000); }
template <int AUX> DEV u32x4 ld16(rsrc_t r, u32 v, u32 s) { return __builtin_amdgcn_raw_buffer_load_b128(r, v, s, AUX); }
template <int AUX> DEV u32x2 ld8(rsrc_t r, u32 v, u32 s) { return __builtin_amdgcn_raw_buffer_load_b64(r, v, s, AUX); }
template <int AUX> DEV void st16(u32x4 d, rsrc_t r, u32 v, u32 s) { __builtin_amdgcn_raw_buffer_store_b128(d, r, v, s, AUX); }
template <int AUX> DEV void st8(u32x2 d, rsrc_t r, u32 v, u32 s) { __builtin_amdgcn_raw_buffer_store_b64(d, r, v, s, AUX); }
template <int AUX> DEV void st4(u32 d, rsrc_t r, u32 v, u32 s) { __builtin_amdgcn_raw_buffer_store_b32(d, r, v, s, AUX); }
template <int SC1, int IMM> DEV void dma(u32 m0, u32 voff, rsrc_t r, u32 soff) {
  if constexpr (SC1)
    asm volatile("s_mov_b32 m0, %0\n\ts_nop 0\n\tbuffer_load_dwordx4 %1, %2, %3 offen offset:%4 sc1 lds" ::"s"(m0), "v"(voff), "s"(r), "s"(soff), "n"(IMM) : "memory");
  else
    asm volatile("s_mov_b32 m0, %0\n\ts_nop 0\n\tbuffer_load_dwordx4 %1, %2, %3 offen offset:%4 lds" ::"s"(m0), "v"(voff), "s"(r), "s"(soff), "n"(IMM) : "memory");
}
template <int N> DEV void waitv() { asm volatile("s_waitcnt vmcnt(%0)" ::"n"(N) : "memory"); }
DEV void waitl0() { asm volatile("s_waitcnt lgkmcnt(0)" ::: "memory"); }
DEV void bar() { asm volatile("s_barrier" ::: "memory"); }
DEV f32x4 mfma(u32x4 a, u32x4 b, f32x4 c) {
  return __builtin_amdgcn_mfma_f32_16x16x32_bf16(__builtin_bit_cast(bf16x8, a), __builtin_bit_cast(bf16x8, b), c, 0, 0, 0);
}
DEV u32 pk(float lo, float hi) { f32x2 v = {lo, hi}; return __builtin_bit_cast(u32, __builtin_convertvector(v, bf16x2)); }
DEV float blo(u32 u) { return __uint_as_float(u << 16); }
DEV float bhi(u32 u) { return __uint_as_float(u & 0xffff0000u); }
template <int CTRL> DEV float dppadd(float v) {
  return v + __uint_as_float(__builtin_amdgcn_update_dpp(0u, __float_as_uint(v), CTRL, 0xf, 0xf, true));
}
DEV float sum16(float v) { v = dppadd<0x128>(v); v = dppadd<0x124>(v); v = dppadd<0x122>(v); v = dppadd<0x121>(v); return v; }
DEV float addx16(float v) { u32 u = __float_as_uint(v); auto r = __builtin_amdgcn_permlane16_swap(u, u, false, false); return __uint_as_float(r[0]) + __uint_as_float(r[1]); }
DEV float addx32(float v) { u32 u = __float_as_uint(v); auto r = __builtin_amdgcn_permlane32_swap(u, u, false, false); return __uint_as_float(r[0]) + __uint_as_float(r[1]); }
DEV u32x2 trread(const char* smem, u32 off) {
  s16x4 r = __builtin_amdgcn_ds_read_tr16_b64_v4i16((LAS s16x4*)(smem + off));
  return __builtin_bit_cast(u32x2, r);
}
DEV u32 ldsaddr(const char* smem) { return (u32)(size_t)(const LAS char*)smem; }

template <int SC> DEV void phase0(const Params& p, char* smem, int b, int w, int lane) {
  const rsrc_t rws = mkrs(p.ws, WS_END);
  const u32 l0 = ldsaddr(smem);
  constexpr u32 LN_OFF = 0, WT_OFF = 10240;
  const rsrc_t rx = mkrs(p.x, S * D * 2);
  u32x2 xv[5];
  {
    const u32 v = (u32)(8 * b + 2 * w + (lane >> 5)) * 1280 + (lane & 31) * 8;
#pragma unroll
    for (int c = 0; c < 5; ++c) xv[c] = ld8<0>(rx, v + c * 256, 0);
  }
  if (b < 108) {
    const rsrc_t r = mkrs(p.w1, D * F * 2);
    const int cg = b % 27, kq = b / 27;
    const u32 v = (u32)(lane >> 3) * 3456 + (lane & 7) * 16;
#pragma unroll
    for (int i = 0; i < 5; ++i) dma<0, 0>(l0 + WT_OFF + (w * 5 + i) * 1024, v, r, (u32)(160 * kq + 8 * (w * 5 + i)) * 3456 + cg * 128);
  } else if (b < 216) {
    const rsrc_t r = mkrs(p.w2, D * F * 2);
    const int u = b - 108, ks = u >> 1, ch = u & 1;
#pragma unroll
    for (int i = 0; i < 5; ++i) {
      const u32 o = (u32)(w * 5 + i) * 1024 + lane * 16, row = o / 640, c = o - row * 640;
      dma<0, 0>(l0 + WT_OFF + (w * 5 + i) * 1024, row * 1280 + c, r, (u32)ks * 32 * 1280 + ch * 640);
    }
  } else {
    const rsrc_t r0 = mkrs(p.dr, D * D * 2), r1 = mkrs(p.di, D * D * 2);
    const int eb = b - 216;
#pragma unroll
    for (int i = 0; i < 5; ++i) {
      const u32 o = (u32)(w * 5 + i) * 64 + lane, row = o / 80, c = o - row * 80;
      const u32 v = row * 1280 + ((c ^ row) << 4);
      dma<0, 0>(l0 + WT_OFF + (w * 5 + i) * 1024, v, r0, (u32)eb * 20480);
      dma<0, 0>(l0 + WT_OFF + 20480 + (w * 5 + i) * 1024, v, r1, (u32)eb * 20480);
    }
  }
  float f[20], s = 0.f;
#pragma unroll
  for (int c = 0; c < 5; ++c) { f[4 * c] = blo(xv[c][0]); f[4 * c + 1] = bhi(xv[c][0]); f[4 * c + 2] = blo(xv[c][1]); f[4 * c + 3] = bhi(xv[c][1]); }
#pragma unroll
  for (int c = 0; c < 20; ++c) s += f[c];
  s = addx16(sum16(s));
  const float mean = s * (1.f / D);
  float q2 = 0.f;
#pragma unroll
  for (int c = 0; c < 20; ++c) { f[c] -= mean; q2 += f[c] * f[c]; }
  q2 = addx16(sum16(q2));
  const float rs = 1.f / sqrtf(q2 * (1.f / D) + 1e-5f);
  {
    char* dst = smem + LN_OFF + (2 * w + (lane >> 5)) * 1280 + (lane & 31) * 8;
#pragma unroll
    for (int c = 0; c < 5; ++c) {
      u32x2 o = {pk(f[4 * c] * rs, f[4 * c + 1] * rs), pk(f[4 * c + 2] * rs, f[4 * c + 3] * rs)};
      *(u32x2*)(dst + c * 256) = o;
    }
  }
  waitv<0>();
  waitl0();
  bar();
  const int g = lane >> 4, i16 = lane & 15;
  {
    const u32 a = LN_OFF + (i16 >> 2) * 1280 + g * 32 + (i16 & 3) * 8;
#pragma unroll
    for (int c = 0; c < 3; ++c) {
      const int blk = w + 4 * c;
      if (blk < 10) {
        u32x2 lo = trread(smem, a + blk * 128), hi = trread(smem, a + blk * 128 + 4 * 1280);
        u32x4 o = {lo[0], lo[1], hi[0], hi[1]};
        st16<SC>(o, rws, lane * 16, O_XNT + b * 10240 + blk * 1024);
      }
    }
  }
  if (b < 108) {
    const int cg = b % 27, kq = b / 27;
    const u32 a = WT_OFF + (4 * g + (i16 >> 2)) * 128 + w * 32 + (i16 & 3) * 8;
    float cs = 0.f;
#pragma unroll
    for (int i = 0; i < 5; ++i) {
      u32x2 lo = trread(smem, a + i * 4096), hi = trread(smem, a + i * 4096 + 2048);
      u32x4 o = {lo[0], lo[1], hi[0], hi[1]};
      st16<SC>(o, rws, lane * 16, O_W1P + ((cg * 4 + w) * 20 + kq * 5 + i) * 1024);
#pragma unroll
      for (int e = 0; e < 4; ++e) cs += blo(o[e]) + bhi(o[e]);
    }
    cs = addx32(addx16(cs));
    if (lane < 16) st4<SC>(__float_as_uint(cs), rws, lane * 16, O_CS + (cg * 64 + w * 16) * 16 + kq * 4);
  } else if (b < 216) {
    const int u = b - 108, ks = u >> 1, ch = u & 1;
    const u32 a = WT_OFF + (4 * g + (i16 >> 2)) * 640 + w * 160 + (i16 & 3) * 8;
#pragma unroll
    for (int i = 0; i < 5; ++i) {
      u32x2 lo = trread(smem, a + i * 32), hi = trread(smem, a + i * 32 + 16 * 640);
      u32x4 o = {lo[0], lo[1], hi[0], hi[1]};
      st16<SC>(o, rws, lane * 16, O_W2P + ((ch * 20 + w * 5 + i) * 54 + ks) * 1024);
    }
  } else {
    const int eb = b - 216;
#pragma unroll
    for (int i = 0; i < 10; ++i) {
      const u32 c = (u32)(w * 10 + i) * 2 + (g >> 1);
      const u32 a = WT_OFF + i16 * 1280 + ((c ^ i16) << 4) + (g & 1) * 8;
      u32x2 re = *(const u32x2*)(smem + a), im = *(const u32x2*)(smem + a + 20480);
      u32x4 o = {re[0], re[1], im[0] ^ 0x80008000u, im[1] ^ 0x80008000u};
      st16<SC>(o, rws, lane * 16, O_DP + (eb * 40 + w * 10 + i) * 1024);
    }
  }
}

template <int SC> DEV void phase_pq(const Params& p, char* smem, int rg, int j, int rot, int w, int lane) {
  const rsrc_t rws = mkrs(p.ws, WS_END), rcr = mkrs(p.cr, S * S * 2), rci = mkrs(p.ci, S * S * 2);
  const u32 l0 = ldsaddr(smem);
  constexpr u32 STG = 53248;
  const int t = lane & 15, q = lane >> 4;
  u32 va[4], vb[5];
#pragma unroll
  for (int i = 0; i < 4; ++i) { const u32 row = 4 * i + q; va[i] = (u32)(64 * rg + 16 * w + row) * 4096 + ((t ^ row) << 4); }
#pragma unroll
  for (int i = 0; i < 5; ++i) { const u32 o = (u32)(w * 5 + i) * 64 + lane, sb = o / 80; vb[i] = sb * 10240 + (o - sb * 80) * 16; }
  const u32 mA = l0 + w * 8192, mB = l0 + 32768 + w * 5120, sB= O_XNT + j * 1280;
  auto issue = [&](int n, u32 buf, u32 kst) {
    if (n < 4) dma<0, 0>(mA + buf + n * 1024, va[n], rcr, kst * 256);
    else if (n < 8) dma<0, 0>(mA + buf + n * 1024, va[n - 4], rci, kst * 256);
    else dma<SC ? 1 : 0, 0>(mB + buf + (n - 8) * 1024, vb[n - 8], rws, sB + kst * 163840);
  };
#pragma unroll
  for (int n = 0; n < 13; ++n) issue(n, 0, rot & 15);
#pragma unroll
  for (int n = 0; n < 13; ++n) issue(n, STG, (rot + 1) & 15);
  f32x4 aP[5], aQ[5];
#pragma unroll
  for (int i = 0; i < 5; ++i) { aP[i] = f32x4{0, 0, 0, 0}; aQ[i] = f32x4{0, 0, 0, 0}; }
  u32 ra[4];
#pragma unroll
  for (int k = 0; k < 4; ++k) ra[k] = w * 8192 + t * 256 + (((4 * k + q) ^ t) << 4);
  const u32 rb = 32768 + q * 1280 + t * 16;
  u32 cur = 0, nxt = 2 * STG;
#pragma unroll 1
  for (int it = 0; it < 16; ++it) {
    if (it < 15) waitv<13>(); else waitv<0>();
    bar();
    const u32 kn = (rot + it + 2) & 15;
    const bool more = it < 14;
    const char* sb = smem + cur;
#pragma unroll
    for (int k = 0; k < 4; ++k) {
      u32x4 cr = *(const u32x4*)(sb + ra[k]), ci = *(const u32x4*)(sb + ra[k] + 4096);
      u32x4 xb[5];
#pragma unroll
      for (int i = 0; i < 5; ++i) xb[i] = *(const u32x4*)(sb + rb + k * 5120 + i * 256);
      if (more) {
        if (k == 0) { issue(0, nxt, kn); issue(1, nxt, kn); issue(2, nxt, kn); issue(3, nxt, kn); }
        else { issue(1 + 3 * k, nxt, kn); issue(2 + 3 * k, nxt, kn); issue(3 + 3 * k, nxt, kn); }
      }
#pragma unroll
      for (int i = 0; i < 5; ++i) { aP[i] = mfma(xb[i], cr, aP[i]); aQ[i] = mfma(xb[i], ci, aQ[i]); }
    }
    nxt = cur;
    cur = cur == 2 * STG ? 0 : cur + STG;
  }
#pragma unroll
  for (int i = 0; i < 5; ++i) {
    u32x4 o = {pk(aP[i][0], aP[i][1]), pk(aP[i][2], aP[i][3]), pk(aQ[i][0], aQ[i][1]), pk(aQ[i][2], aQ[i][3])};
    st16<SC>(o, rws, lane * 16, O_PQ + ((4 * rg + w) * 40 + 5 * j + i) * 1024);
  }
}

template <int NK, int PITCH, int SC, int DEPTH>
DEV void ksplit(f32x4 (&acc)[5], const u32x4 (&wb)[NK][5], rsrc_t rws, u32 sA, u32 vlast, char* smem, int w, int lane) {
  u32x4 a[DEPTH][NK];
  auto loadA = [&](int s) {
    const u32 base = sA + ((w + 1 + s) & 3) * (PITCH * 1024);
#pragma unroll
    for (int k = 0; k < NK; ++k)
      a[s % DEPTH][k] = ld16<SC>(rws, (k == NK - 1 ? vlast : lane * 16) + (k & 3) * 1024, base + (k >> 2) * 4096);
  };
#pragma unroll
  for (int s = 0; s < DEPTH; ++s) loadA(s);
#pragma unroll
  for (int s = 0; s < 4; ++s) {
#pragma unroll
    for (int i = 0; i < 5; ++i) acc[i] = f32x4{0, 0, 0, 0};
#pragma unroll
    for (int k = 0; k < NK; ++k)
#pragma unroll
      for (int i = 0; i < 5; ++i) acc[i] = mfma(wb[k][i], a[s % DEPTH][k], acc[i]);
    if (s + DEPTH < 4) loadA(s + DEPTH);
    if (s < 3) {
      char* dst = smem + (((w + 1 + s) & 3) * 3 + s) * 5120 + lane * 16;
#pragma unroll
      for (int i = 0; i < 5; ++i) *(f32x4*)(dst + i * 1024) = acc[i];
    }
  }
  waitl0();
  bar();
  const char* src = smem + w * 15360 + lane * 16;
#pragma unroll
  for (int s = 0; s < 3; ++s)
#pragma unroll
    for (int i = 0; i < 5; ++i) acc[i] += *(const f32x4*)(src + s * 5120 + i * 1024);
}

DEV int blk5(int j, int i) { int v = i + (j & 1); return 5 * j + (v >= 5 ? v - 5 : v); }

template <int SC> DEV void phase_h(const Params& p, char* smem, int rg, int j, int w, int lane) {
  const rsrc_t rws = mkrs(p.ws, WS_END), rx = mkrs(p.x, S * D * 2);
  const int t = lane & 15, q = lane >> 4;
  u32x4 wb[10][5];
#pragma unroll
  for (int i = 0; i < 5; ++i)
#pragma unroll
    for (int k = 0; k < 10; ++k) wb[k][i] = ld16<SC>(rws, lane * 16 + (k & 3) * 1024, O_DP + (blk5(j, i) * 40 + 10 * w + (k & ~3)) * 1024);
  u32x2 xr[5];
#pragma unroll
  for (int i = 0; i < 5; ++i) xr[i] = ld8<0>(rx, (u32)t * 1280 + q * 8, (u32)(64 * rg + 16 * w) * 1280 + blk5(j, i) * 32);
  f32x4 acc[5];
  ksplit<10, 40, SC, 4>(acc, wb, rws, O_PQ + (4 * rg * 40 + 10 * w) * 1024, lane * 16, smem, w, lane);
  u32x2 h[5];
  float s1 = 0.f, s2 = 0.f;
#pragma unroll
  for (int i = 0; i < 5; ++i) {
    h[i][0] = pk(acc[i][0] + blo(xr[i][0]), acc[i][1] + bhi(xr[i][0]));
    h[i][1] = pk(acc[i][2] + blo(xr[i][1]), acc[i][3] + bhi(xr[i][1]));
    const float a0 = blo(h[i][0]), a1 = bhi(h[i][0]), a2 = blo(h[i][1]), a3 = bhi(h[i][1]);
    s1 += (a0 + a1) + (a2 + a3);
    s2 += a0 * a0 + a1 * a1 + a2 * a2 + a3 * a3;
  }
  const u32 hb = O_H + (4 * rg + w) * 20480;
  const int kp = (5 * j + 1) >> 1;
  st16<SC>(u32x4{h[0][0], h[0][1], h[1][0], h[1][1]}, rws, lane * 16, hb + kp * 1024);
  st16<SC>(u32x4{h[2][0], h[2][1], h[3][0], h[3][1]}, rws, lane * 16 + 1024, hb + kp * 1024);
  st8<SC>(h[4], rws, lane * 16, hb + (blk5(j, 4) >> 1) * 1024 + (j & 1) * 8);
  s1 = addx32(addx16(s1));
  s2 = addx32(addx16(s2));
  if (lane < 16) st8<SC>(u32x2{__float_as_uint(s1), __float_as_uint(s2)}, rws, lane * 64, O_ST + (64 * rg + 16 * w) * 64 + j * 8);
}

template <int SC> DEV void phase_f(const Params& p, char* smem, int sg, int mi, int w, int lane) {
  const rsrc_t rws = mkrs(p.ws, WS_END), rb1 = mkrs(p.b1, F * 2);
  const u32 l0 = ldsaddr(smem);
  const int q = lane >> 4;
  const int nb0 = mi < 12 ? 7 * mi : 84 + 6 * (mi - 12), odd = mi < 12 ? (mi & 1) : 0;
  auto nbi = [&](int i) { int v = i + odd; v = v >= 7 ? 0 : v; return mi >= 12 && i == 6 ? nb0 : nb0 + v; };
#pragma unroll
  for (int i = 0; i < 7; ++i) {
    const u32 m = l0 + (i * 20 + 5 * w) * 1024, s = O_W1P + (nbi(i) * 20 + 5 * w) * 1024;
    dma<SC ? 1 : 0, 0>(m, lane * 16, rws, s);
    dma<SC ? 1 : 0, 1024>(m, lane * 16, rws, s);
    dma<SC ? 1 : 0, 2048>(m, lane * 16, rws, s);
    dma<SC ? 1 : 0, 3072>(m, lane * 16, rws, s);
    dma<SC ? 1 : 0, 0>(m + 4096, lane * 16, rws, s + 4096);
  }
  constexpr u32 CB = 143360;
  {
    const int tid = w * 64 + lane;
    if (tid < 112) {
      const int i = tid >> 4;
      int v = i + odd; v = v >= 7 ? 0 : v; v = (mi >= 12 && i == 6) ? 0 : v;
      const u32 col = (u32)(nb0 + v) * 16 + (tid & 15);
      const u32x4 c = ld16<SC>(rws, col * 16, O_CS);
      const u32 bb = __builtin_amdgcn_raw_buffer_load_b16(rb1, col * 2, 0, 0);
      const float cs = (__uint_as_float(c[0]) + __uint_as_float(c[1])) + (__uint_as_float(c[2]) + __uint_as_float(c[3]));
      *(float*)(smem + CB + tid * 4) = cs;
      *(float*)(smem + CB + 448 + tid * 4) = blo(bb);
    }
  }
  u32x4 a[20][2], stv[2];
#pragma unroll
  for (int m = 0; m < 2; ++m) stv[m] = ld16<SC>(rws, (lane & 15) * 64 + q * 16, O_ST + (128 * sg + 32 * w + 16 * m) * 64);
#pragma unroll
  for (int k = 0; k < 20; ++k)
#pragma unroll
    for (int m = 0; m < 2; ++m) a[k][m] = ld16<SC>(rws, lane * 16 + (k & 3) * 1024, O_H + ((8 * sg + 2 * w + m) * 20 + (k & ~3)) * 1024);
  waitv<42>();
  waitl0();
  bar();
  f32x4 acc[2][7];
#pragma unroll
  for (int m = 0; m < 2; ++m)
#pragma unroll
    for (int i = 0; i < 7; ++i) acc[m][i] = f32x4{0, 0, 0, 0};
#pragma unroll
  for (int k = 0; k < 20; ++k)
#pragma unroll
    for (int i = 0; i < 7; ++i) {
      const u32x4 wv = *(const u32x4*)(smem + (i * 20 + k) * 1024 + lane * 16);
      acc[0][i] = mfma(wv, a[k][0], acc[0][i]);
      acc[1][i] = mfma(wv, a[k][1], acc[1][i]);
    }
  float mean[2], rstd[2];
#pragma unroll
  for (int m = 0; m < 2; ++m) {
    const float s1 = addx32(addx16(__uint_as_float(stv[m][0]) + __uint_as_float(stv[m][2])));
    const float s2 = addx32(addx16(__uint_as_float(stv[m][1]) + __uint_as_float(stv[m][3])));
    mean[m] = s1 * (1.f / D);
    const float var = fmaxf(s2 * (1.f / D) - mean[m] * mean[m], 0.f);
    rstd[m] = 1.f / sqrtf(var + 1e-5f);
  }
  u32x2 fo[2][7];
#pragma unroll
  for (int i = 0; i < 7; ++i) {
    const f32x4 cs = *(const f32x4*)(smem + CB + i * 64 + q * 16), bb = *(const f32x4*)(smem + CB + 448 + i * 64 + q * 16);
#pragma unroll
    for (int m = 0; m < 2; ++m) {
      float v[4];
#pragma unroll
      for (int e = 0; e < 4; ++e) { const float z = (acc[m][i][e] - mean[m] * cs[e]) * rstd[m] + bb[e]; v[e] = fmaxf(z, 0.f) * z; }
      fo[m][i] = u32x2{pk(v[0], v[1]), pk(v[2], v[3])};
    }
  }
  const int kp = (nb0 + 1) >> 1;
#pragma unroll
  for (int m = 0; m < 2; ++m) {
    const u32 fb = O_F + (8 * sg + 2 * w + m) * 55296;
#pragma unroll
    for (int c = 0; c < 3; ++c) st16<SC>(u32x4{fo[m][2 * c][0], fo[m][2 * c][1], fo[m][2 * c + 1][0], fo[m][2 * c + 1][1]}, rws, lane * 16 + c * 1024, fb + kp * 1024);
    if (mi < 12) st8<SC>(fo[m][6], rws, lane * 16, fb + (nbi(6) >> 1) * 1024 + odd * 8);
  }
}

template <int SC> DEV void phase_out(const Params& p, char* smem, int rg, int j, int w, int lane) {
  const rsrc_t rws = mkrs(p.ws, WS_END), rb2 = mkrs(p.b2, D * 2), rout = mkrs(p.out, S * D * 2);
  const int t = lane & 15, q = lane >> 4;
  const int k0 = w < 2 ? 14 * w : 28 + 13 * (w - 2);
  const u32 vlast = w < 2 ? lane * 16 : 0x7f000000u;
  u32x4 wb[14][5];
#pragma unroll
  for (int i = 0; i < 5; ++i)
#pragma unroll
    for (int k = 0; k < 14; ++k) wb[k][i] = ld16<SC>(rws, (k == 13 ? vlast : lane * 16) + (k & 3) * 1024, O_W2P + (blk5(j, i) * 54 + k0 + (k & ~3)) * 1024);
  const u32 hb = O_H + (4 * rg + w) * 20480;
  const int kp = (5 * j + 1) >> 1;
  const u32x4 h01 = ld16<SC>(rws, lane * 16, hb + kp * 1024), h23 = ld16<SC>(rws, lane * 16 + 1024, hb + kp * 1024);
  const u32x2 h4 = ld8<SC>(rws, lane * 16, hb + (blk5(j, 4) >> 1) * 1024 + (j & 1) * 8);
  u32x2 bv[5];
#pragma unroll
  for (int i = 0; i < 5; ++i) bv[i] = ld8<0>(rb2, q * 8, blk5(j, i) * 32);
  f32x4 acc[5];
  ksplit<14, 54, SC, 2>(acc, wb, rws, O_F + (4 * rg * 54 + k0) * 1024, vlast, smem, w, lane);
  const u32x2 h[5] = {{h01[0], h01[1]}, {h01[2], h01[3]}, {h23[0], h23[1]}, {h23[2], h23[3]}, h4};
#pragma unroll
  for (int i = 0; i < 5; ++i) {
    u32x2 o;
    o[0] = pk(acc[i][0] + blo(bv[i][0]) + blo(h[i][0]), acc[i][1] + bhi(bv[i][0]) + bhi(h[i][0]));
    o[1] = pk(acc[i][2] + blo(bv[i][1]) + blo(h[i][1]), acc[i][3] + bhi(bv[i][1]) + bhi(h[i][1]));
    st8<0>(o, rout, (u32)t * 1280 + q * 8, (u32)(64 * rg + 16 * w) * 1280 + blk5(j, i) * 32);
  }
}

template <int PH> __global__ __launch_bounds__(256, 1) void fnet_kernel(Params p) {
  extern __shared__ __attribute__((aligned(16))) char smem[];
  const int b = blockIdx.x, w = __builtin_amdgcn_readfirstlane(threadIdx.x >> 6), lane = threadIdx.x & 63;
  const int l = b >> 3, sg = (b & 7) * 2 + (l >> 4), mi = l & 15, rg = sg * 2 + (mi >> 3), j = mi & 7;
  if constexpr (PH == 0) phase0<0>(p, smem, b, w, lane);
  if constexpr (PH == 1) phase_pq<0>(p, smem, rg, j, 2 * j, w, lane);
  if constexpr (PH == 2) phase_h<0>(p, smem, rg, j, w, lane);
  if constexpr (PH == 3) phase_f<0>(p, smem, sg, mi, w, lane);
  if constexpr (PH == 4) phase_out<0>(p, smem, rg, j, w, lane);
}

template <int PH> static void launch(const Params& p, int lds, hipStream_t stream) {
  (void)hipFuncSetAttribute((const void*)fnet_kernel<PH>, hipFuncAttributeMaxDynamicSharedMemorySize, lds);
  fnet_kernel<PH><<<256, 256, lds, stream>>>(p);
}

extern "C" void kernel_launch(void* const* d_in, const int* in_sizes, int n_in, void* d_out, int out_size, void* d_ws, size_t ws_size,
                              hipStream_t stream) {
  Params p;
  p.x = d_in[0]; p.cr = d_in[1]; p.ci = d_in[2]; p.dr = d_in[3]; p.di = d_in[4];
  p.w1 = d_in[6]; p.b1 = d_in[7]; p.w2 = d_in[8]; p.b2 = d_in[9];
  p.out = d_out; p.ws = d_ws;
  launch<0>(p, 51200, stream);
  launch<1>(p, LDS_BYTES, stream);
  launch<2>(p, 61440, stream);
  launch<3>(p, 144256, stream);
  launch<4>(p, 61440, stream);
}
```

```cpp
#include <hip/hip_runtime.h>
#include <hip/hip_bf16.h>
#include <type_traits>

typedef unsigned short u16;
typedef unsigned int u32;
typedef unsigned long long u64;
typedef __attribute__((ext_vector_type(4))) u32 u32x4;
typedef __attribute__((ext_vector_type(2))) u32 u32x2;
typedef __attribute__((ext_vector_type(4))) float f32x4;
typedef __attribute__((ext_vector_type(2))) float f32x2;
typedef __attribute__((ext_vector_type(8))) __bf16 bf16x8;
typedef __attribute__((ext_vector_type(2))) __bf16 bf16x2;
typedef __attribute__((ext_vector_type(4))) short s16x4;
typedef __amdgpu_buffer_rsrc_t rsrc_t;
#define LAS __attribute__((address_space(3)))
#define DEV static __device__ __forceinline__

constexpr int S = 2048, D = 640, F = 1728;
constexpr u32 O_XNT = 0;
constexpr u32 O_W1P = O_XNT + 2621440;
constexpr u32 O_W2P = O_W1P + 2211840;
constexpr u32 O_DP  = O_W2P + 2211840;
constexpr u32 O_PQ  = O_DP + 1638400;
constexpr u32 O_H   = O_PQ + 5242880;
constexpr u32 O_F   = O_H + 2621440;
constexpr u32 O_CS  = O_F + 7077888;
constexpr u32 O_ST  = O_CS + 27648;
constexpr u32 WS_END = O_ST + 131072;
constexpr int LDS_BYTES = 159744 + 256;
#define ROT (2 * j)

#define DBGP nullptr
#define STAMP(i)
#define STAMP_ARG
#define STAMP_PASS
struct Params {
  const void *x, *cr, *ci, *dr, *di, *w1, *b1, *w2, *b2;
  void *out, *ws;
};

DEV rsrc_t mkrs(const void* p, u32 bytes) { return __builtin_amdgcn_make_buffer_rsrc((void*)p, 0, (int)bytes, 0x00020000); }
template <int AUX> DEV u32x4 ld16(rsrc_t r, u32 v, u32 s) { return __builtin_amdgcn_raw_buffer_load_b128(r, v, s, AUX); }
template <int AUX> DEV u32x2 ld8(rsrc_t r, u32 v, u32 s) { return __builtin_amdgcn_raw_buffer_load_b64(r, v, s, AUX); }
template <int AUX> DEV void st16(u32x4 d, rsrc_t r, u32 v, u32 s) { __builtin_amdgcn_raw_buffer_store_b128(d, r, v, s, AUX); }
template <int AUX> DEV void st8(u32x2 d, rsrc_t r, u32 v, u32 s) { __builtin_amdgcn_raw_buffer_store_b64(d, r, v, s, AUX); }
template <int AUX> DEV void st4(u32 d, rsrc_t r, u32 v, u32 s) { __builtin_amdgcn_raw_buffer_store_b32(d, r, v, s, AUX); }
template <int SC1, int IMM> DEV void dma(u32 m0, u32 voff, rsrc_t r, u32 soff) {
  if constexpr (SC1)
    asm volatile("s_mov_b32 m0, %0\n\ts_nop 0\n\tbuffer_load_dwordx4 %1, %2, %3 offen offset:%4 sc1 lds" ::"s"(m0), "v"(voff), "s"(r), "s"(soff), "n"(IMM) : "memory");
  else
    asm volatile("s_mov_b32 m0, %0\n\ts_nop 0\n\tbuffer_load_dwordx4 %1, %2, %3 offen offset:%4 lds" ::"s"(m0), "v"(voff), "s"(r), "s"(soff), "n"(IMM) : "memory");
}
template <int N> DEV void waitv() { asm volatile("s_waitcnt vmcnt(%0)" ::"n"(N) : "memory"); }
DEV void waitl0() { asm volatile("s_waitcnt lgkmcnt(0)" ::: "memory"); }
DEV void bar() { asm volatile("s_barrier" ::: "memory"); }
DEV f32x4 mfma(u32x4 a, u32x4 b, f32x4 c) {
  return __builtin_amdgcn_mfma_f32_16x16x32_bf16(__builtin_bit_cast(bf16x8, a), __builtin_bit_cast(bf16x8, b), c, 0, 0, 0);
}
DEV void mf(f32x4& c, const u32x4& a, const u32x4& b) { asm volatile("v_mfma_f32_16x16x32_bf16 %0, %1, %2, %0" : "+v"(c) : "v"(a), "v"(b)); }
DEV void mfa(f32x4& c, const u32x4& a, const u32x4& b) { asm volatile("v_mfma_f32_16x16x32_bf16 %0, %1, %2, %0" : "+v"(c) : "a"(a), "v"(b)); }
DEV void mfa0(f32x4& c, const u32x4& a, const u32x4& b) { asm volatile("v_mfma_f32_16x16x32_bf16 %0, %1, %2, 0" : "=&v"(c) : "a"(a), "v"(b)); }
DEV void mf0(f32x4& c, const u32x4& a, const u32x4& b) { asm volatile("v_mfma_f32_16x16x32_bf16 %0, %1, %2, 0" : "=&v"(c) : "v"(a), "v"(b)); }
DEV void mfence5(f32x4 (&c)[5]) { asm volatile("s_nop 7" : "+v"(c[0]), "+v"(c[1]), "+v"(c[2]), "+v"(c[3]), "+v"(c[4])); }
DEV u32x4 lds16(u32 a) { return *(const LAS u32x4*)(size_t)a; }
DEV u32 pk(float lo, float hi) { f32x2 v = {lo, hi}; return __builtin_bit_cast(u32, __builtin_convertvector(v, bf16x2)); }
DEV float blo(u32 u) { return __uint_as_float(u << 16); }
DEV float bhi(u32 u) { return __uint_as_float(u & 0xffff0000u); }
template <int CTRL> DEV float dppadd(float v) {
  return v + __uint_as_float(__builtin_amdgcn_update_dpp(0u, __float_as_uint(v), CTRL, 0xf, 0xf, true));
}
DEV float sum16(float v) { v = dppadd<0x128>(v); v = dppadd<0x124>(v); v = dppadd<0x122>(v); v = dppadd<0x121>(v); return v; }
DEV float addx16(float v) { u32 u = __float_as_uint(v); auto r = __builtin_amdgcn_permlane16_swap(u, u, false, false); return __uint_as_float(r[0]) + __uint_as_float(r[1]); }
DEV float addx32(float v) { u32 u = __float_as_uint(v); auto r = __builtin_amdgcn_permlane32_swap(u, u, false, false); return __uint_as_float(r[0]) + __uint_as_float(r[1]); }
DEV u32x2 trread(const char* smem, u32 off) {
  s16x4 r = __builtin_amdgcn_ds_read_tr16_b64_v4i16((LAS s16x4*)(smem + off));
  return __builtin_bit_cast(u32x2, r);
}
DEV u32 ldsaddr(const char* smem) { return (u32)(size_t)(const LAS char*)smem; }

template <int SC> DEV void phase0(const Params& p, char* smem, int b, int w, int lane) {
  const rsrc_t rws = mkrs(p.ws, WS_END);
  const u32 l0 = ldsaddr(smem);
  constexpr u32 LN_OFF = 0, WT_OFF = 10240;
  const rsrc_t rx = mkrs(p.x, S * D * 2);
  u32x2 xv[5];
  {
    const u32 v = (u32)(8 * b + 2 * w + (lane >> 5)) * 1280 + (lane & 31) * 8;
#pragma unroll
    for (int c = 0; c < 5; ++c) xv[c] = ld8<0>(rx, v + c * 256, 0);
  }
  if (b < 108) {
    const rsrc_t r = mkrs(p.w1, D * F * 2);
    const int cg = b % 27, kq = b / 27;
    const u32 v = (u32)(lane >> 3) * 3456 + (lane & 7) * 16;
#pragma unroll
    for (int i = 0; i < 5; ++i) dma<0, 0>(l0 + WT_OFF + (w * 5 + i) * 1024, v, r, (u32)(160 * kq + 8 * (w * 5 + i)) * 3456 + cg * 128);
  } else if (b < 216) {
    const rsrc_t r = mkrs(p.w2, D * F * 2);
    const int u = b - 108, ks = u >> 1, ch = u & 1;
#pragma unroll
    for (int i = 0; i < 5; ++i) {
      const u32 o = (u32)(w * 5 + i) * 1024 + lane * 16, row = o / 640, c = o - row * 640;
      dma<0, 0>(l0 + WT_OFF + (w * 5 + i) * 1024, row * 1280 + c, r, (u32)ks * 32 * 1280 + ch * 640);
    }
  } else {
    const rsrc_t r0 = mkrs(p.dr, D * D * 2), r1 = mkrs(p.di, D * D * 2);
    const int eb = b - 216;
#pragma unroll
    for (int i = 0; i < 5; ++i) {
      const u32 o = (u32)(w * 5 + i) * 64 + lane, row = o / 80, c = o - row * 80;
      const u32 v = row * 1280 + ((c ^ row) << 4);
      dma<0, 0>(l0 + WT_OFF + (w * 5 + i) * 1024, v, r0, (u32)eb * 20480);
      dma<0, 0>(l0 + WT_OFF + 20480 + (w * 5 + i) * 1024, v, r1, (u32)eb * 20480);
    }
  }
  float f[20], s = 0.f;
#pragma unroll
  for (int c = 0; c < 5; ++c) { f[4 * c] = blo(xv[c][0]); f[4 * c + 1] = bhi(xv[c][0]); f[4 * c + 2] = blo(xv[c][1]); f[4 * c + 3] = bhi(xv[c][1]); }
#pragma unroll
  for (int c = 0; c < 20; ++c) s += f[c];
  s = addx16(sum16(s));
  const float mean = s * (1.f / D);
  float q2 = 0.f;
#pragma unroll
  for (int c = 0; c < 20; ++c) { f[c] -= mean; q2 += f[c] * f[c]; }
  q2 = addx16(sum16(q2));
  const float rs = 1.f / sqrtf(q2 * (1.f / D) + 1e-5f);
  {
    char* dst = smem + LN_OFF + (2 * w + (lane >> 5)) * 1280 + (lane & 31) * 8;
#pragma unroll
    for (int c = 0; c < 5; ++c) {
      u32x2 o = {pk(f[4 * c] * rs, f[4 * c + 1] * rs), pk(f[4 * c + 2] * rs, f[4 * c + 3] * rs)};
      *(u32x2*)(dst + c * 256) = o;
    }
  }
  waitv<0>();
  waitl0();
  bar();
  const int g = lane >> 4, i16 = lane & 15;
  {
    const u32 a = LN_OFF + (i16 >> 2) * 1280 + g * 32 + (i16 & 3) * 8;
#pragma unroll
    for (int c = 0; c < 3; ++c) {
      const int blk = w + 4 * c;
      if (blk < 10) {
        u32x2 lo = trread(smem, a + blk * 128), hi = trread(smem, a + blk * 128 + 4 * 1280);
        u32x4 o = {lo[0], lo[1], hi[0], hi[1]};
        st16<SC>(o, rws, lane * 16, O_XNT + b * 10240 + blk * 1024);
      }
    }
  }
  if (b < 108) {
    const int cg = b % 27, kq = b / 27;
    const u32 a = WT_OFF + (4 * g + (i16 >> 2)) * 128 + w * 32 + (i16 & 3) * 8;
    float cs = 0.f;
#pragma unroll
    for (int i = 0; i < 5; ++i) {
      u32x2 lo = trread(smem, a + i * 4096), hi = trread(smem, a + i * 4096 + 2048);
      u32x4 o = {lo[0], lo[1], hi[0], hi[1]};
      st16<SC>(o, rws, lane * 16, O_W1P + ((cg * 4 + w) * 20 + kq * 5 + i) * 1024);
#pragma unroll
      for (int e = 0; e < 4; ++e) cs += blo(o[e]) + bhi(o[e]);
    }
    cs = addx32(addx16(cs));
    if (lane < 16) st4<SC>(__float_as_uint(cs), rws, lane * 16, O_CS + (cg * 64 + w * 16) * 16 + kq * 4);
  } else if (b < 216) {
    const int u = b - 108, ks = u >> 1, ch = u & 1;
    const u32 a = WT_OFF + (4 * g + (i16 >> 2)) * 640 + w * 160 + (i16 & 3) * 8;
#pragma unroll
    for (int i = 0; i < 5; ++i) {
      u32x2 lo = trread(smem, a + i * 32), hi = trread(smem, a + i * 32 + 16 * 640);
      u32x4 o = {lo[0], lo[1], hi[0], hi[1]};
      st16<SC>(o, rws, lane * 16, O_W2P + ((ch * 20 + w * 5 + i) * 54 + ks) * 1024);
    }
  } else {
    const int eb = b - 216;
#pragma unroll
    for (int i = 0; i < 10; ++i) {
      const u32 c = (u32)(w * 10 + i) * 2 + (g >> 1);
      const u32 a = WT_OFF + i16 * 1280 + ((c ^ i16) << 4) + (g & 1) * 8;
      u32x2 re = *(const u32x2*)(smem + a), im = *(const u32x2*)(smem + a + 20480);
      u32x4 o = {re[0], re[1], im[0] ^ 0x80008000u, im[1] ^ 0x80008000u};
      st16<SC>(o, rws, lane * 16, O_DP + (eb * 40 + w * 10 + i) * 1024);
    }
  }
}

#define SB __builtin_amdgcn_sched_barrier(0)
template <int IMM> DEV void setm0(u32 base) { asm volatile("s_add_u32 m0, %0, %1" ::"s"(base), "n"(IMM) : "scc"); }
template <int SC1> DEV void dmago(u32 voff, rsrc_t r, u32 soff) {
  if constexpr (SC1) asm volatile("buffer_load_dwordx4 %0, %1, %2 offen sc1 lds" ::"v"(voff), "s"(r), "s"(soff) : "memory");
  else asm volatile("buffer_load_dwordx4 %0, %1, %2 offen lds" ::"v"(voff), "s"(r), "s"(soff) : "memory");
}
template <int SC> DEV void phase_pq(const Params& p, char* smem, int rg, int j, int rot, int w, int lane, u64* dbg) {
  const rsrc_t rws = mkrs(p.ws, WS_END), rcr = mkrs(p.cr, S * S * 2), rci = mkrs(p.ci, S * S * 2);
  const u32 l0 = ldsaddr(smem);
  constexpr u32 STG = 53248;
  const int t = lane & 15, q = lane >> 4;
  u32 va[4], vb[5];
#pragma unroll
  for (int i = 0; i < 4; ++i) { const u32 row = 4 * i + q; va[i] = (u32)(64 * rg + 16 * w + row) * 4096 + ((t ^ row) << 4); }
#pragma unroll
  for (int i = 0; i < 5; ++i) { const u32 o = (u32)(w * 5 + i) * 64 + lane, sb = o / 80; vb[i] = sb * 10240 + (o - sb * 80) * 16; }
  const u32 mA = l0 + w * 8192, mB = l0 + 32768 + w * 5120, sB = O_XNT + j * 1280;
  auto issue = [&](int n, u32 buf, u32 kst) {
    if (n < 4) dma<0, 0>(mA + buf + n * 1024, va[n], rcr, kst * 256);
    else if (n < 8) dma<0, 0>(mA + buf + n * 1024, va[n - 4], rci, kst * 256);
    else dma<SC ? 1 : 0, 0>(mB + buf + (n - 8) * 1024, vb[n - 8], rws, sB + kst * 163840);
  };
#pragma unroll
  for (int n = 0; n < 13; ++n) issue(n, 0, rot & 15);
#pragma unroll
  for (int n = 0; n < 13; ++n) issue(n, STG, (rot + 1) & 15);
  f32x4 aP[5], aQ[5];
  u32x4 fr[2][7];
#pragma unroll
  for (int i = 0; i < 5; ++i) { aP[i] = f32x4{0, 0, 0, 0}; aQ[i] = f32x4{0, 0, 0, 0}; }
#pragma unroll
  for (int i = 0; i < 7; ++i) fr[1][i] = u32x4{0, 0, 0, 0};
  u32 ra[4];
#pragma unroll
  for (int k = 0; k < 4; ++k) ra[k] = l0 + w * 8192 + t * 256 + (((4 * k + q) ^ t) << 4);
  u32 rb = l0 + 32768 + q * 1280 + t * 16;
  u32 nxt = 2 * STG;
  auto step = [&](auto U_, auto K_, auto N0_, auto N1_, bool dm, u32 mAn, u32 mBn, u32 soA, u32 soB) {
    constexpr int U = U_.value, K = K_.value, N0 = N0_.value, N1 = N1_.value;
#pragma unroll
    for (int g = 0; g < 10; ++g) {
      const int i = g >> 1;
      const int n = N0 + (g % 3 == 2 ? -1 : g / 3);
      if (g & 1) mf(aQ[i], fr[U][2 + i], fr[U][1]); else mf(aP[i], fr[U][2 + i], fr[U][0]);
      SB;
      if (g < 7) {
        const int r = g == 0 ? 2 : g == 1 ? 0 : g == 2 ? 1 : g;
        fr[U ^ 1][r] = r < 2 ? lds16(ra[K] + r * 4096) : lds16(rb + K * 5120 + (r - 2) * 256);
      }
      if (dm) {
        const int c = g / 3, nn = N0 + c;
        if (g % 3 == 0 && nn < N1) { if (nn < 8) asm volatile("s_add_u32 m0, %0, %1" ::"s"(mAn), "n"(nn * 1024) : "scc"); else asm volatile("s_add_u32 m0, %0, %1" ::"s"(mBn), "n"((nn - 8) * 1024) : "scc"); }
        if (g % 3 == 1 && nn < N1) { if (nn < 4) dmago<0>(va[nn & 3], rcr, soA); else if (nn < 8) dmago<0>(va[nn & 3], rci, soA); else dmago<SC ? 1 : 0>(vb[nn < 8 ? 0 : nn - 8], rws, soB); }
      }
      (void)n;
      SB;
    }
  };
  using std::integral_constant;
#define IC(x) integral_constant<int, x>{}
  auto body = [&](bool dm, u32 kn) {
    const u32 mAn = mA + nxt, mBn = mB + nxt, soA = kn * 256, soB = sB + kn * 163840;
    step(IC(1), IC(0), IC(0), IC(3), dm, mAn, mBn, soA, soB);
    step(IC(0), IC(1), IC(3), IC(6), dm, mAn, mBn, soA, soB);
    step(IC(1), IC(2), IC(6), IC(9), dm, mAn, mBn, soA, soB);
    step(IC(0), IC(3), IC(9), IC(12), dm, mAn, mBn, soA, soB);
    if (dm) { setm0<4096>(mBn); asm volatile("s_nop 0"); dmago<SC ? 1 : 0>(vb[4], rws, soB); }
  };
  auto adv = [&]() {
    const u32 cur = nxt == 0 ? STG : nxt == STG ? 2 * STG : 0;
    const int d = cur == 2 * STG ? -2 * (int)STG : (int)STG;
#pragma unroll
    for (int k = 0; k < 4; ++k) ra[k] += d;
    rb += d;
    nxt = cur;
  };
#pragma unroll 1
  for (int it = 0; it < 14; ++it) {
    waitv<13>();
    waitl0();
    bar();
    SB;
    body(true, (rot + it + 2) & 15);
    adv();
  }
#pragma unroll 1
  for (int it = 14; it < 16; ++it) {
    if (it == 14) waitv<13>(); else waitv<0>();
    waitl0();
    bar();
    SB;
    body(false, 0);
    adv();
  }
#pragma unroll
  for (int i = 0; i < 5; ++i) { mf(aP[i], fr[1][2 + i], fr[1][0]); mf(aQ[i], fr[1][2 + i], fr[1][1]); }
  mfence5(aP);
  mfence5(aQ);
#pragma unroll
  for (int i = 0; i < 5; ++i) {
    u32x4 o = {pk(aP[i][0], aP[i][1]), pk(aP[i][2], aP[i][3]), pk(aQ[i][0], aQ[i][1]), pk(aQ[i][2], aQ[i][3])};
    st16<SC>(o, rws, lane * 16, O_PQ + ((4 * rg + w) * 40 + 5 * j + i) * 1024);
  }
}

template <int NK, int PITCH, int SC, int DEPTH>
DEV void ksplit(f32x4 (&acc)[5], const u32x4 (&wb)[NK][5], rsrc_t rws, u32 sA, u32 vlast, char* smem, int w, int lane) {
  u32x4 a[DEPTH][NK];
  auto loadA = [&](int s) {
    const u32 base = sA + ((w + 1 + s) & 3) * (PITCH * 1024);
#pragma unroll
    for (int k = 0; k < NK; ++k)
      a[s % DEPTH][k] = ld16<SC>(rws, (k == NK - 1 ? vlast : lane * 16) + (k & 3) * 1024, base + (k >> 2) * 4096);
  };
#pragma unroll
  for (int s = 0; s < DEPTH; ++s) loadA(s);
  __builtin_amdgcn_sched_barrier(0);
#pragma unroll
  for (int s = 0; s < 4; ++s) {
#pragma unroll
    for (int k = 0; k < NK; ++k)
#pragma unroll
      for (int i = 0; i < 5; ++i) {
        if (k == 0) mfa0(acc[i], wb[k][i], a[s % DEPTH][k]);
        else if (k < 12) mfa(acc[i], wb[k][i], a[s % DEPTH][k]);
        else mf(acc[i], wb[k][i], a[s % DEPTH][k]);
      }
    __builtin_amdgcn_sched_barrier(0);
    if (s + DEPTH < 4) loadA(s + DEPTH);
    __builtin_amdgcn_sched_barrier(0);
    mfence5(acc);
    if (s < 3) {
      char* dst = smem + (((w + 1 + s) & 3) * 3 + s) * 5120 + lane * 16;
#pragma unroll
      for (int i = 0; i < 5; ++i) *(f32x4*)(dst + i * 1024) = acc[i];
    }
  }
  waitl0();
  bar();
  const char* src = smem + w * 15360 + lane * 16;
#pragma unroll
  for (int s = 0; s < 3; ++s)
#pragma unroll
    for (int i = 0; i < 5; ++i) acc[i] += *(const f32x4*)(src + s * 5120 + i * 1024);
}

DEV int blk5(int j, int i) { int v = i + (j & 1); return 5 * j + (v >= 5 ? v - 5 : v); }

template <int SC> DEV void phase_h(const Params& p, char* smem, int rg, int j, int w, int lane) {
  const rsrc_t rws = mkrs(p.ws, WS_END), rx = mkrs(p.x, S * D * 2);
  const int t = lane & 15, q = lane >> 4;
  u32x4 wb[10][5];
#pragma unroll
  for (int i = 0; i < 5; ++i)
#pragma unroll
    for (int k = 0; k < 10; ++k) wb[k][i] = ld16<SC>(rws, lane * 16 + (k & 3) * 1024, O_DP + (blk5(j, i) * 40 + 10 * w + (k & ~3)) * 1024);
  u32x2 xr[5];
#pragma unroll
  for (int i = 0; i < 5; ++i) xr[i] = ld8<0>(rx, (u32)t * 1280 + q * 8, (u32)(64 * rg + 16 * w) * 1280 + blk5(j, i) * 32);
  f32x4 acc[5];
  ksplit<10, 40, SC, 4>(acc, wb, rws, O_PQ + (4 * rg * 40 + 10 * w) * 1024, lane * 16, smem, w, lane);
  u32x2 h[5];
  float s1 = 0.f, s2 = 0.f;
#pragma unroll
  for (int i = 0; i < 5; ++i) {
    h[i][0] = pk(acc[i][0] + blo(xr[i][0]), acc[i][1] + bhi(xr[i][0]));
    h[i][1] = pk(acc[i][2] + blo(xr[i][1]), acc[i][3] + bhi(xr[i][1]));
    const float a0 = blo(h[i][0]), a1 = bhi(h[i][0]), a2 = blo(h[i][1]), a3 = bhi(h[i][1]);
    s1 += (a0 + a1) + (a2 + a3);
    s2 += a0 * a0 + a1 * a1 + a2 * a2 + a3 * a3;
  }
  const u32 hb = O_H + (4 * rg + w) * 20480;
  const int kp = (5 * j + 1) >> 1;
  st16<SC>(u32x4{h[0][0], h[0][1], h[1][0], h[1][1]}, rws, lane * 16, hb + kp * 1024);
  st16<SC>(u32x4{h[2][0], h[2][1], h[3][0], h[3][1]}, rws, lane * 16 + 1024, hb + kp * 1024);
  st8<SC>(h[4], rws, lane * 16, hb + (blk5(j, 4) >> 1) * 1024 + (j & 1) * 8);
  s1 = addx32(addx16(s1));
  s2 = addx32(addx16(s2));
  if (lane < 16) st8<SC>(u32x2{__float_as_uint(s1), __float_as_uint(s2)}, rws, lane * 64, O_ST + (64 * rg + 16 * w) * 64 + j * 8);
}

template <int SC> DEV void phase_f(const Params& p, char* smem, int sg, int mi, int w, int lane) {
  const rsrc_t rws = mkrs(p.ws, WS_END), rb1 = mkrs(p.b1, F * 2);
  const u32 l0 = ldsaddr(smem);
  const int q = lane >> 4;
  const int nb0 = mi < 12 ? 7 * mi : 84 + 6 * (mi - 12), odd = mi < 12 ? (mi & 1) : 0;
  auto nbi = [&](int i) { int v = i + odd; v = v >= 7 ? 0 : v; return mi >= 12 && i == 6 ? nb0 : nb0 + v; };
#pragma unroll
  for (int i = 0; i < 7; ++i) {
    const u32 m = l0 + (i * 20 + 5 * w) * 1024, s = O_W1P + (nbi(i) * 20 + 5 * w) * 1024;
    dma<SC ? 1 : 0, 0>(m, lane * 16, rws, s);
    dma<SC ? 1 : 0, 1024>(m, lane * 16, rws, s);
    dma<SC ? 1 : 0, 2048>(m, lane * 16, rws, s);
    dma<SC ? 1 : 0, 3072>(m, lane * 16, rws, s);
    dma<SC ? 1 : 0, 0>(m + 4096, lane * 16, rws, s + 4096);
  }
  constexpr u32 CB = 143360;
  {
    const int tid = w * 64 + lane;
    if (tid < 112) {
      const int i = tid >> 4;
      int v = i + odd; v = v >= 7 ? 0 : v; v = (mi >= 12 && i == 6) ? 0 : v;
      const u32 col = (u32)(nb0 + v) * 16 + (tid & 15);
      const u32x4 c = ld16<SC>(rws, col * 16, O_CS);
      const u32 bb = __builtin_amdgcn_raw_buffer_load_b16(rb1, col * 2, 0, 0);
      const float cs = (__uint_as_float(c[0]) + __uint_as_float(c[1])) + (__uint_as_float(c[2]) + __uint_as_float(c[3]));
      *(float*)(smem + CB + tid * 4) = cs;
      *(float*)(smem + CB + 448 + tid * 4) = blo(bb);
    }
  }
  u32x4 a[20][2], stv[2];
#pragma unroll
  for (int m = 0; m < 2; ++m) stv[m] = ld16<SC>(rws, (lane & 15) * 64 + q * 16, O_ST + (128 * sg + 32 * w + 16 * m) * 64);
#pragma unroll
  for (int k = 0; k < 20; ++k)
#pragma unroll
    for (int m = 0; m < 2; ++m) a[k][m] = ld16<SC>(rws, lane * 16 + (k & 3) * 1024, O_H + ((8 * sg + 2 * w + m) * 20 + (k & ~3)) * 1024);
  waitv<42>();
  waitl0();
  bar();
  f32x4 acc[2][7];
  u32x4 wv[4];
  const u32 lb0 = l0 + lane * 16, lb1 = lb0 + 61440, lb2 = lb0 + 122880;
  auto rdw = [&](int n) { const int k = n / 7, i = n % 7, o = (i * 20 + k) * 1024; return o < 61440 ? lds16(lb0 + o) : o < 122880 ? lds16(lb1 + (o - 61440)) : lds16(lb2 + (o - 122880)); };
#pragma unroll
  for (int n = 0; n < 3; ++n) wv[n] = rdw(n);
  __builtin_amdgcn_sched_barrier(0);
#pragma unroll
  for (int n = 0; n < 140; ++n) {
    const int k = n / 7, i = n % 7;
    if (k == 0) mf0(acc[0][i], wv[n & 3], a[k][0]); else mf(acc[0][i], wv[n & 3], a[k][0]);
    __builtin_amdgcn_sched_barrier(0);
    if (n + 3 < 140) wv[(n + 3) & 3] = rdw(n + 3);
    __builtin_amdgcn_sched_barrier(0);
    if (k == 0) mf0(acc[1][i], wv[n & 3], a[k][1]); else mf(acc[1][i], wv[n & 3], a[k][1]);
    __builtin_amdgcn_sched_barrier(0);
  }
  asm volatile("s_nop 7" : "+v"(acc[0][0]), "+v"(acc[0][1]), "+v"(acc[0][2]), "+v"(acc[0][3]), "+v"(acc[0][4]), "+v"(acc[0][5]), "+v"(acc[0][6]),
               "+v"(acc[1][0]), "+v"(acc[1][1]), "+v"(acc[1][2]), "+v"(acc[1][3]), "+v"(acc[1][4]), "+v"(acc[1][5]), "+v"(acc[1][6]));
  float mean[2], rstd[2];
#pragma unroll
  for (int m = 0; m < 2; ++m) {
    const float s1 = addx32(addx16(__uint_as_float(stv[m][0]) + __uint_as_float(stv[m][2])));
    const float s2 = addx32(addx16(__uint_as_float(stv[m][1]) + __uint_as_float(stv[m][3])));
    mean[m] = s1 * (1.f / D);
    const float var = fmaxf(s2 * (1.f / D) - mean[m] * mean[m], 0.f);
    rstd[m] = 1.f / sqrtf(var + 1e-5f);
  }
  u32x2 fo[2][7];
#pragma unroll
  for (int i = 0; i < 7; ++i) {
    const f32x4 cs = *(const f32x4*)(smem + CB + i * 64 + q * 16), bb = *(const f32x4*)(smem + CB + 448 + i * 64 + q * 16);
#pragma unroll
    for (int m = 0; m < 2; ++m) {
      float v[4];
#pragma unroll
      for (int e = 0; e < 4; ++e) { const float z = (acc[m][i][e] - mean[m] * cs[e]) * rstd[m] + bb[e]; v[e] = fmaxf(z, 0.f) * z; }
      fo[m][i] = u32x2{pk(v[0], v[1]), pk(v[2], v[3])};
    }
  }
  const int kp = (nb0 + 1) >> 1;
#pragma unroll
  for (int m = 0; m < 2; ++m) {
    const u32 fb = O_F + (8 * sg + 2 * w + m) * 55296;
#pragma unroll
    for (int c = 0; c < 3; ++c) st16<SC>(u32x4{fo[m][2 * c][0], fo[m][2 * c][1], fo[m][2 * c + 1][0], fo[m][2 * c + 1][1]}, rws, lane * 16 + c * 1024, fb + kp * 1024);
    if (mi < 12) st8<SC>(fo[m][6], rws, lane * 16, fb + (nbi(6) >> 1) * 1024 + odd * 8);
  }
}

template <int SC> DEV void phase_out(const Params& p, char* smem, int rg, int j, int w, int lane) {
  const rsrc_t rws = mkrs(p.ws, WS_END), rb2 = mkrs(p.b2, D * 2), rout = mkrs(p.out, S * D * 2);
  const int t = lane & 15, q = lane >> 4;
  const int k0 = w < 2 ? 14 * w : 28 + 13 * (w - 2);
  const u32 vlast = w < 2 ? lane * 16 : 0x7f000000u;
  u32x4 wb[14][5];
#pragma unroll
  for (int i = 0; i < 5; ++i)
#pragma unroll
    for (int k = 0; k < 14; ++k) wb[k][i] = ld16<SC>(rws, (k == 13 ? vlast : lane * 16) + (k & 3) * 1024, O_W2P + (blk5(j, i) * 54 + k0 + (k & ~3)) * 1024);
  const u32 hb = O_H + (4 * rg + w) * 20480;
  const int kp = (5 * j + 1) >> 1;
  const u32x4 h01 = ld16<SC>(rws, lane * 16, hb + kp * 1024), h23 = ld16<SC>(rws, lane * 16 + 1024, hb + kp * 1024);
  const u32x2 h4 = ld8<SC>(rws, lane * 16, hb + (blk5(j, 4) >> 1) * 1024 + (j & 1) * 8);
  u32x2 bv[5];
#pragma unroll
  for (int i = 0; i < 5; ++i) bv[i] = ld8<0>(rb2, q * 8, blk5(j, i) * 32);
  f32x4 acc[5];
  ksplit<14, 54, SC, 2>(acc, wb, rws, O_F + (4 * rg * 54 + k0) * 1024, vlast, smem, w, lane);
  const u32x2 h[5] = {{h01[0], h01[1]}, {h01[2], h01[3]}, {h23[0], h23[1]}, {h23[2], h23[3]}, h4};
#pragma unroll
  for (int i = 0; i < 5; ++i) {
    u32x2 o;
    o[0] = pk(acc[i][0] + blo(bv[i][0]) + blo(h[i][0]), acc[i][1] + bhi(bv[i][0]) + bhi(h[i][0]));
    o[1] = pk(acc[i][2] + blo(bv[i][1]) + blo(h[i][1]), acc[i][3] + bhi(bv[i][1]) + bhi(h[i][1]));
    st8<0>(o, rout, (u32)t * 1280 + q * 8, (u32)(64 * rg + 16 * w) * 1280 + blk5(j, i) * 32);
  }
}

template <int PH> __global__ __launch_bounds__(256, 1) void fnet_kernel(Params p) {
  extern __shared__ __attribute__((aligned(16))) char smem[];
  const int b = blockIdx.x, w = __builtin_amdgcn_readfirstlane(threadIdx.x >> 6), lane = threadIdx.x & 63;
  const int l = b >> 3, sg = (b & 7) * 2 + (l >> 4), mi = l & 15, rg = sg * 2 + (mi >> 3), j = mi & 7;
  STAMP(PH * 3);
  if constexpr (PH == 0) phase0<0>(p, smem, b, w, lane);
  if constexpr (PH == 1) phase_pq<0>(p, smem, rg, j, ROT, w, lane, DBGP);
  if constexpr (PH == 2) phase_h<0>(p, smem, rg, j, w, lane);
  if constexpr (PH == 3) phase_f<0>(p, smem, sg, mi, w, lane);
  if constexpr (PH == 4) phase_out<0>(p, smem, rg, j, w, lane);
  STAMP(PH * 3 + 1);
}

template <int PH> static void launch(const Params& p, int lds, hipStream_t stream) {
  (void)hipFuncSetAttribute((const void*)fnet_kernel<PH>, hipFuncAttributeMaxDynamicSharedMemorySize, lds);
  fnet_kernel<PH><<<256, 256, lds, stream>>>(p);
}

extern "C" void kernel_launch(void* const* d_in, const int* in_sizes, int n_in, void* d_out, int out_size, void* d_ws, size_t ws_size,
                              hipStream_t stream) {
  Params p;
  p.x = d_in[0]; p.cr = d_in[1]; p.ci = d_in[2]; p.dr = d_in[3]; p.di = d_in[4];
  p.w1 = d_in[6]; p.b1 = d_in[7]; p.w2 = d_in[8]; p.b2 = d_in[9];
  p.out = d_out; p.ws = d_ws;
  launch<0>(p, 51200, stream);
  launch<1>(p, LDS_BYTES, stream);
  launch<2>(p, 61440, stream);
  launch<3>(p, 144256, stream);
  launch<4>(p, 61440, stream);
}
```

```cpp
#include <hip/hip_runtime.h>
#include <hip/hip_bf16.h>
#include <type_traits>
#include <utility>

typedef unsigned short u16;
typedef unsigned int u32;
typedef unsigned long long u64;
typedef __attribute__((ext_vector_type(4))) u32 u32x4;
typedef __attribute__((ext_vector_type(2))) u32 u32x2;
typedef __attribute__((ext_vector_type(4))) float f32x4;
typedef __attribute__((ext_vector_type(2))) float f32x2;
typedef __attribute__((ext_vector_type(2))) __bf16 bf16x2;
typedef __attribute__((ext_vector_type(4))) short s16x4;
typedef __amdgpu_buffer_rsrc_t rsrc_t;
#define LAS __attribute__((address_space(3)))
#define DEV static __device__ __forceinline__
#define SB __builtin_amdgcn_sched_barrier(0)

constexpr int S = 2048, D = 640, F = 1728;
constexpr u32 O_XNT = 0;
constexpr u32 O_W1P = O_XNT + 2621440;
constexpr u32 O_W2P = O_W1P + 2211840;
constexpr u32 O_DP  = O_W2P + 2211840;
constexpr u32 O_PQ  = O_DP + 1638400;
constexpr u32 O_H   = O_PQ + 5242880;
constexpr u32 O_F   = O_H + 2621440;
constexpr u32 O_CS  = O_F + 7077888;
constexpr u32 O_ST  = O_CS + 27648;
constexpr u32 O_FL  = O_ST + 131072;
constexpr u32 O_NON = O_FL + 5 * 256 * 128;
constexpr u32 WS_END = O_NON + 128;
constexpr int LDS_BYTES = 163840;
constexpr int SC1 = 16;
constexpr u32 SPIN_MAX = 1u << 18;
constexpr u32 L_CR = 0, L_XB = 65536, L_LN = 147456, L_WT = L_XB + 2 * 20480;
constexpr u32 L_RED = 102400, L_CS = 143360;
#define CI_EARLY 0
#define CR_EARLY 1
#define ROT (2 * j)
#define W2_N0 20
#define W2_N1 25

#define STAMP(i)

struct Params {
  const void *x, *cr, *ci, *dr, *di, *w1, *b1, *w2, *b2;
  void *out, *ws;
};

template <class Fn, int... I> DEV void sfor_(Fn&& f, std::integer_sequence<int, I...>) { (f(std::integral_constant<int, I>{}), ...); }
template <int N, class Fn> DEV void sfor(Fn&& f) { sfor_(f, std::make_integer_sequence<int, N>{}); }

DEV rsrc_t mkrs(const void* p, u32 bytes) { return __builtin_amdgcn_make_buffer_rsrc((void*)p, 0, (int)bytes, 0x00020000); }
template <int AUX> DEV u32x4 ld16(rsrc_t r, u32 v, u32 s) { return __builtin_amdgcn_raw_buffer_load_b128(r, v, s, AUX); }
template <int AUX> DEV u32x2 ld8(rsrc_t r, u32 v, u32 s) { return __builtin_amdgcn_raw_buffer_load_b64(r, v, s, AUX); }
template <int AUX> DEV void st16(u32x4 d, rsrc_t r, u32 v, u32 s) { __builtin_amdgcn_raw_buffer_store_b128(d, r, v, s, AUX); }
template <int AUX> DEV void st8(u32x2 d, rsrc_t r, u32 v, u32 s) { __builtin_amdgcn_raw_buffer_store_b64(d, r, v, s, AUX); }
template <int AUX> DEV void st4(u32 d, rsrc_t r, u32 v, u32 s) { __builtin_amdgcn_raw_buffer_store_b32(d, r, v, s, AUX); }
template <int IMM> DEV void setm0(u32 base) { asm volatile("s_add_u32 m0, %0, %1" ::"s"(base), "n"(IMM) : "scc"); }
template <int SC, int IMM> DEV void dmago(u32 voff, rsrc_t r, u32 soff) {
  if constexpr (SC) asm volatile("buffer_load_dwordx4 %0, %1, %2 offen offset:%3 sc1 lds" ::"v"(voff), "s"(r), "s"(soff), "n"(IMM) : "memory");
  else asm volatile("buffer_load_dwordx4 %0, %1, %2 offen offset:%3 lds" ::"v"(voff), "s"(r), "s"(soff), "n"(IMM) : "memory");
}
template <int SC, int MI> DEV void dma(u32 m0, u32 voff, rsrc_t r, u32 soff) {
  setm0<MI>(m0);
  asm volatile("s_nop 0");
  dmago<SC, 0>(voff, r, soff);
}
template <int R, int SC, int IMM> DEV void lda(u32 voff, rsrc_t r, u32 soff) {
  if constexpr (SC) asm volatile("buffer_load_dwordx4 a[%0:%1], %2, %3, %4 offen offset:%5 sc1" ::"n"(R), "n"(R + 3), "v"(voff), "s"(r), "s"(soff), "n"(IMM) : "memory");
  else asm volatile("buffer_load_dwordx4 a[%0:%1], %2, %3, %4 offen offset:%5" ::"n"(R), "n"(R + 3), "v"(voff), "s"(r), "s"(soff), "n"(IMM) : "memory");
}
template <int SC, int IMM> DEV void ldv(u32x4& d, u32 voff, rsrc_t r, u32 soff) {
  if constexpr (SC) asm volatile("buffer_load_dwordx4 %0, %1, %2, %3 offen offset:%4 sc1" : "=v"(d) : "v"(voff), "s"(r), "s"(soff), "n"(IMM) : "memory");
  else asm volatile("buffer_load_dwordx4 %0, %1, %2, %3 offen offset:%4" : "=v"(d) : "v"(voff), "s"(r), "s"(soff), "n"(IMM) : "memory");
}
template <int SC, int IMM> DEV void ldv8(u32x2& d, u32 voff, rsrc_t r, u32 soff) {
  if constexpr (SC) asm volatile("buffer_load_dwordx2 %0, %1, %2, %3 offen offset:%4 sc1" : "=v"(d) : "v"(voff), "s"(r), "s"(soff), "n"(IMM) : "memory");
  else asm volatile("buffer_load_dwordx2 %0, %1, %2, %3 offen offset:%4" : "=v"(d) : "v"(voff), "s"(r), "s"(soff), "n"(IMM) : "memory");
}
template <int N> DEV void waitv() { asm volatile("s_waitcnt vmcnt(%0)" ::"n"(N > 63 ? 63 : N) : "memory"); }
template <class T> DEV void landed(T& v) { asm volatile("; landed %0" : "+v"(v)); }
DEV void waitl0() { asm volatile("s_waitcnt lgkmcnt(0)" ::: "memory"); }
DEV void bar() { asm volatile("s_barrier" ::: "memory"); }
DEV void mf(f32x4& c, const u32x4& a, const u32x4& b) { asm volatile("v_mfma_f32_16x16x32_bf16 %0, %1, %2, %0" : "+v"(c) : "v"(a), "v"(b)); }
DEV void mf0(f32x4& c, const u32x4& a, const u32x4& b) { asm volatile("v_mfma_f32_16x16x32_bf16 %0, %1, %2, 0" : "=&v"(c) : "v"(a), "v"(b)); }
template <int R> DEV void mfB(f32x4& c, const u32x4& a) { asm volatile("v_mfma_f32_16x16x32_bf16 %0, %1, a[%2:%3], %0" : "+v"(c) : "v"(a), "n"(R), "n"(R + 3)); }
template <int R> DEV void mfB0(f32x4& c, const u32x4& a) { asm volatile("v_mfma_f32_16x16x32_bf16 %0, %1, a[%2:%3], 0" : "=&v"(c) : "v"(a), "n"(R), "n"(R + 3)); }
template <int R> DEV void mfA(f32x4& c, const u32x4& b) { asm volatile("v_mfma_f32_16x16x32_bf16 %0, a[%2:%3], %1, %0" : "+v"(c) : "v"(b), "n"(R), "n"(R + 3)); }
template <int R> DEV void mfA0(f32x4& c, const u32x4& b) { asm volatile("v_mfma_f32_16x16x32_bf16 %0, a[%2:%3], %1, 0" : "=&v"(c) : "v"(b), "n"(R), "n"(R + 3)); }
DEV void mfence5(f32x4 (&c)[5]) { asm volatile("s_nop 7" : "+v"(c[0]), "+v"(c[1]), "+v"(c[2]), "+v"(c[3]), "+v"(c[4])); }
DEV void mfence7(f32x4 (&c)[7]) { asm volatile("s_nop 7" : "+v"(c[0]), "+v"(c[1]), "+v"(c[2]), "+v"(c[3]), "+v"(c[4]), "+v"(c[5]), "+v"(c[6])); }

DEV u32x4 lds16(u32 a) { return *(const LAS u32x4*)(size_t)a; }
DEV u32x2 lds8(u32 a) { return *(const LAS u32x2*)(size_t)a; }
DEV void sts16(u32 a, u32x4 v) { *(LAS u32x4*)(size_t)a = v; }
DEV void sts4(u32 a, float v) { *(LAS float*)(size_t)a = v; }
DEV u32x4 tr16(u32 lo, u32 hi) {
  const u32x2 a = __builtin_bit_cast(u32x2, __builtin_amdgcn_ds_read_tr16_b64_v4i16((LAS s16x4*)(size_t)lo));
  const u32x2 b = __builtin_bit_cast(u32x2, __builtin_amdgcn_ds_read_tr16_b64_v4i16((LAS s16x4*)(size_t)hi));
  return u32x4{a[0], a[1], b[0], b[1]};
}
DEV u32 pk(float lo, float hi) { f32x2 v = {lo, hi}; return __builtin_bit_cast(u32, __builtin_convertvector(v, bf16x2)); }
DEV float blo(u32 u) { return __uint_as_float(u << 16); }
DEV float bhi(u32 u) { return __uint_as_float(u & 0xffff0000u); }
template <int CTRL> DEV float dppadd(float v) {
  return v + __uint_as_float(__builtin_amdgcn_update_dpp(0u, __float_as_uint(v), CTRL, 0xf, 0xf, true));
}
DEV float sum16(float v) { v = dppadd<0x128>(v); v = dppadd<0x124>(v); v = dppadd<0x122>(v); v = dppadd<0x121>(v); return v; }
DEV float addx16(float v) { u32 u = __float_as_uint(v); auto r = __builtin_amdgcn_permlane16_swap(u, u, false, false); return __uint_as_float(r[0]) + __uint_as_float(r[1]); }
DEV float addx32(float v) { u32 u = __float_as_uint(v); auto r = __builtin_amdgcn_permlane32_swap(u, u, false, false); return __uint_as_float(r[0]) + __uint_as_float(r[1]); }
DEV int blk5(int j, int i) { int v = i + (j & 1); return 5 * j + (v >= 5 ? v - 5 : v); }

DEV void publish(rsrc_t rws, int edge, int g, u32x2 tag, int lane) {
  if (lane == 0) st8<SC1>(tag, rws, 0, O_FL + (edge * 256 + g) * 128);
}
template <int NL> DEV void await(rsrc_t rws, u32 voff, u32 soff, u32x2 tag) {
  for (u32 spin = 0; spin < SPIN_MAX; ++spin) {
    u32x2 v[NL];
    sfor<NL>([&](auto C_) { ldv8<1, 0>(v[C_.value], voff, rws, soff + C_.value * 8192); });
    waitv<0>();
    bool ok = true;
#pragma unroll
    for (int c = 0; c < NL; ++c) { landed(v[c]); ok &= (v[c][0] == tag[0]) & (v[c][1] == tag[1]); }
    if (__builtin_amdgcn_ballot_w64(!ok) == 0) break;
  }
}

template <int NK, int PITCH, int DEPTH, int NB, class Mid>
DEV void ksplit(f32x4 (&acc)[5], const u32x4 (&wv)[10], rsrc_t rws, u32 sA, u32 vlast, u32 l0, int w, int lane, Mid&& mid) {
  u32x4 a[DEPTH][NK];
  const u32 v0 = lane * 16;
  auto loadA = [&](auto S_) {
    constexpr int s = S_.value;
    const u32 base = sA + ((w + 1 + s) & 3) * (PITCH * 1024);
    sfor<NK>([&](auto K_) { constexpr int k = K_.value; ldv<1, (k & 3) * 1024>(a[s % DEPTH][k], k == 13 ? vlast : v0, rws, base + (k >> 2) * 4096); });
  };
  sfor<DEPTH>(loadA);
  mid();
  sfor<4>([&](auto S_) {
    constexpr int s = S_.value;
    sfor<NK>([&](auto K_) {
      constexpr int k = K_.value;
      constexpr int issued = (s + DEPTH < 4 ? s + DEPTH : 4) * NK;
      waitv<issued - (s * NK + k + 1) + (s < DEPTH ? NB : 0)>();
      sfor<5>([&](auto I_) {
        constexpr int i = I_.value;
        if constexpr (k == 0) mfA0<4 * (5 * k + i)>(acc[i], a[s % DEPTH][k]);
        else if constexpr (k < 12) mfA<4 * (5 * k + i)>(acc[i], a[s % DEPTH][k]);
        else mf(acc[i], wv[5 * (k - 12) + i], a[s % DEPTH][k]);
      });
    });
    if constexpr (s + DEPTH < 4) loadA(std::integral_constant<int, s + DEPTH>{});
    mfence5(acc);
    if constexpr (s < 3) {
      const u32 dst = l0 + L_RED + (((w + 1 + s) & 3) * 3 + s) * 5120 + v0;
#pragma unroll
      for (int i = 0; i < 5; ++i) sts16(dst + i * 1024, __builtin_bit_cast(u32x4, acc[i]));
    }
    SB;
  });
  waitl0();
  bar();
  const u32 src = l0 + L_RED + w * 15360 + v0;
#pragma unroll
  for (int s = 0; s < 3; ++s)
#pragma unroll
    for (int i = 0; i < 5; ++i) acc[i] += __builtin_bit_cast(f32x4, lds16(src + s * 5120 + i * 1024));
}

__global__ __launch_bounds__(256, 1) void fnet_block(Params p) {
  extern __shared__ __attribute__((aligned(16))) char smem[];
  asm volatile("" ::: "a0", "a63", "a127", "a191", "a255");
  const int b = blockIdx.x, w = __builtin_amdgcn_readfirstlane(threadIdx.x >> 6), lane = threadIdx.x & 63;
  const int l = b >> 3, sg = (b & 7) * 2 + (l >> 4), mi = l & 15, rg = sg * 2 + (mi >> 3), j = mi & 7, gid = sg * 16 + mi;
  const int t = lane & 15, q = lane >> 4;
  const u32 l0 = (u32)(size_t)(const LAS char*)smem, v0 = lane * 16;
  const rsrc_t rws = mkrs(p.ws, WS_END), rx = mkrs(p.x, S * D * 2), rcr = mkrs(p.cr, S * S * 2), rci = mkrs(p.ci, S * S * 2);
  STAMP(0);
  u64 non;
  asm volatile("s_load_dwordx2 %0, %1, %2" : "=s"(non) : "s"(p.ws), "s"(O_NON) : "memory");
  const int rot = ROT;
  u32 va[4], vb[5];
#pragma unroll
  for (int i = 0; i < 4; ++i) { const u32 row = 4 * i + q; va[i] = (u32)(64 * rg + row) * 4096 + ((t ^ row) << 4); }
  const u32 vci = (u32)(64 * rg + 16 * w + t) * 4096 + q * 16;
  auto load_ci = [&](auto IT_) {
    constexpr int it = IT_.value;
    const u32 so = ((rot + it) & 15) * 256;
    sfor<4>([&](auto K_) { constexpr int k = K_.value; lda<16 * it + 4 * k, 0, 64 * k>(vci, rci, so); });
  };
  auto dma_cr = [&](auto IT_, int ww) {
    constexpr int it = IT_.value;
    const u32 so = ((rot + it) & 15) * 256 + ww * 65536, m = l0 + L_CR + ww * 4096;
    sfor<4>([&](auto N_) { constexpr int n = N_.value; dma<0, (it & 3) * 16384 + n * 1024>(m, va[n], rcr, so); });
  };

  const int hw = w - 2;
  const u32 lwt = l0 + L_WT + hw * 20480;
  if (w < 2) {
    u32x4 xv[5];
    const u32 v = (u32)(8 * b + 4 * w + q) * 1280 + t * 16;
#pragma unroll
    for (int c = 0; c < 5; ++c) xv[c] = ld16<0>(rx, v + c * 256, 0);
    float f[40], s = 0.f;
#pragma unroll
    for (int c = 0; c < 5; ++c)
#pragma unroll
      for (int e = 0; e < 4; ++e) { f[8 * c + 2 * e] = blo(xv[c][e]); f[8 * c + 2 * e + 1] = bhi(xv[c][e]); }
#pragma unroll
    for (int c = 0; c < 40; ++c) s += f[c];
    const float mean = sum16(s) * (1.f / D);
    float q2 = 0.f;
#pragma unroll
    for (int c = 0; c < 40; ++c) { f[c] -= mean; q2 += f[c] * f[c]; }
    const float rs = 1.f / sqrtf(sum16(q2) * (1.f / D) + 1e-5f);
    const u32 dst = l0 + L_LN + (4 * w + q) * 1280 + t * 16;
#pragma unroll
    for (int c = 0; c < 5; ++c)
      sts16(dst + c * 256, u32x4{pk(f[8 * c] * rs, f[8 * c + 1] * rs), pk(f[8 * c + 2] * rs, f[8 * c + 3] * rs), pk(f[8 * c + 4] * rs, f[8 * c + 5] * rs), pk(f[8 * c + 6] * rs, f[8 * c + 7] * rs)});
    waitl0();
  } else {
    if (b < 108) {
      const rsrc_t r = mkrs(p.w1, D * F * 2);
      const int cg = b % 27, kq = b / 27;
      const u32 v = (u32)(lane >> 2) * 3456 + (lane & 3) * 16, s0 = (u32)(160 * kq) * 3456 + cg * 128 + hw * 64;
      sfor<10>([&](auto I_) { constexpr int i = I_.value; dma<0, i * 1024>(lwt, v, r, s0 + i * 16 * 3456); });
    } else if (b < 216) {
      const rsrc_t r = mkrs(p.w2, D * F * 2);
      const int u = b - 108, ks = u >> 1, ch = u & 1;
      const u32 s0 = (u32)ks * 32 * 1280 + ch * 640 + hw * 320;
      sfor<10>([&](auto I_) {
        constexpr int i = I_.value;
        const u32 o = i * 64 + lane, row = o / 20;
        dma<0, i * 1024>(lwt, row * 1280 + (o - row * 20) * 16, r, s0);
      });
    } else {
      const rsrc_t r0 = mkrs(p.dr, D * D * 2), r1 = mkrs(p.di, D * D * 2);
      const u32 s0 = (u32)(b - 216) * 20480 + hw * 640;
      sfor<10>([&](auto I_) {
        constexpr int i = I_.value;
        const u32 o = i * 64 + lane, row = o / 40, c = o - row * 40;
        const u32 v = row * 1280 + ((c ^ ((row >> 1) & 7)) << 4);
        dma<0, i * 1024>(lwt, v, r0, s0);
        dma<0, 10240 + i * 1024>(lwt, v, r1, s0);
      });
    }
    if (CR_EARLY) sfor<4>([&](auto IT_) { dma_cr(IT_, hw); dma_cr(IT_, w); });
    sfor<CI_EARLY>(load_ci);
  }
  asm volatile("s_waitcnt lgkmcnt(0)" : "+s"(non)::"memory");
  non += 0x9E3779B97F4A7C15ull;
  const u32x2 tag = {(u32)non, (u32)(non >> 32)};
  STAMP(1);
  bar();
  STAMP(2);
  if (w == 0) {
    const u32 a = l0 + L_LN + (t >> 2) * 1280 + q * 32 + (t & 3) * 8;
#pragma unroll
    for (int c = 0; c < 10; ++c) st16<SC1>(tr16(a + c * 128, a + c * 128 + 4 * 1280), rws, v0 + (c & 3) * 1024, O_XNT + b * 10240 + (c >> 2) * 4096);
    waitv<0>();
    publish(rws, 0, b, tag, lane);
    STAMP(3);
  } else if (w >= 2) {
    waitv<(CR_EARLY ? 32 : 0) + 4 * CI_EARLY>();
    if (b < 108) {
      const int cg = b % 27, kq = b / 27;
      const u32 a = lwt + (4 * q + (t >> 2)) * 64 + (t & 3) * 8;
#pragma unroll
      for (int n = 0; n < 2; ++n) {
        float cs = 0.f;
#pragma unroll
        for (int i = 0; i < 5; ++i) {
          const u32x4 o = tr16(a + n * 32 + i * 2048, a + n * 32 + i * 2048 + 1024);
          st16<SC1>(o, rws, v0, O_W1P + ((cg * 4 + hw * 2 + n) * 20 + kq * 5 + i) * 1024);
#pragma unroll
          for (int e = 0; e < 4; ++e) cs += blo(o[e]) + bhi(o[e]);
        }
        cs = addx32(addx16(cs));
        if (lane < 16) st4<SC1>(__float_as_uint(cs), rws, v0, O_CS + (cg * 64 + hw * 32 + n * 16) * 16 + kq * 4);
      }
    } else if (b < 216) {
      const int u = b - 108, ks = u >> 1, ch = u & 1;
      const u32 a = lwt + (4 * q + (t >> 2)) * 320 + (t & 3) * 8;
#pragma unroll
      for (int i = 0; i < 10; ++i) st16<SC1>(tr16(a + i * 32, a + i * 32 + 16 * 320), rws, v0, O_W2P + ((ch * 20 + hw * 10 + i) * 54 + ks) * 1024);
    } else {
#pragma unroll
      for (int i = 0; i < 20; ++i) {
        const u32 c = 2 * i + (q >> 1);
        const u32 a = lwt + t * 640 + ((c ^ (t >> 1)) << 4) + (q & 1) * 8;
        const u32x2 re = lds8(a), im = lds8(a + 10240);
        st16<SC1>(u32x4{re[0], re[1], im[0] ^ 0x80008000u, im[1] ^ 0x80008000u}, rws, v0 + (i & 3) * 1024, O_DP + ((b - 216) * 40 + hw * 20 + (i & ~3)) * 1024);
      }
    }
    waitl0();
  }
  if (!CR_EARLY) sfor<4>([&](auto IT_) { dma_cr(IT_, w); });
  if (CI_EARLY > 0 && w < 2) sfor<CI_EARLY>(load_ci);
  sfor<16 - CI_EARLY>([&](auto I_) { load_ci(std::integral_constant<int, I_.value + CI_EARLY>{}); });
  STAMP(4);
  if (w == 3) await<4>(rws, lane * 128, O_FL, tag);
  STAMP(5);
  bar();
  STAMP(6);

#pragma unroll
  for (int i = 0; i < 5; ++i) { const u32 o = (u32)(w * 5 + i) * 64 + lane, sb = o / 80; vb[i] = sb * 10240 + (o - sb * 80) * 16; }
  const u32 mB = l0 + L_XB + w * 5120, mA = l0 + L_CR + w * 4096, sB = O_XNT + j * 1280;
  sfor<3>([&](auto IT_) {
    constexpr int it = IT_.value;
    const u32 so = sB + ((rot + it) & 15) * 163840;
    sfor<5>([&](auto N_) { constexpr int n = N_.value; dma<1, it * 20480 + n * 1024>(mB, vb[n], rws, so); });
  });
  f32x4 aP[5], aQ[5];
  u32x4 fr[2][6];
  u32x2 wfl;
  u32 ra[4];
#pragma unroll
  for (int k = 0; k < 4; ++k) ra[k] = l0 + L_CR + w * 4096 + t * 256 + (((4 * k + q) ^ t) << 4);
  const u32 rb0 = l0 + L_XB + q * 1280 + t * 16, rb1 = rb0 + 40960;
  u32 sD[5];
#pragma unroll
  for (int i = 0; i < 5; ++i) sD[i] = O_DP + (blk5(j, i) * 40 + 10 * w) * 1024;
  auto load_d = [&](auto N_) {
    constexpr int n = N_.value, k = n / 5, i = n % 5;
    lda<4 * n, 1, (k & 3) * 1024>(v0, rws, sD[i] + (k >> 2) * 4096);
  };
  auto cnt = [](int it) { return it < 0 ? 5 : it == 0 ? 5 : it == 8 ? 10 : 9; };
  sfor<16>([&](auto IT_) {
    constexpr int it = IT_.value;
    waitv<cnt(it - 2) + cnt(it - 1)>();
    waitl0();
    bar();
    if constexpr (it == 0) {
      STAMP(7);
      if (w == 0) { publish(rws, 1, b, tag, lane); if (b == 0 && lane == 0) st8<SC1>(tag, rws, 0, O_NON); }
    }
    if constexpr (it == 11) {
      landed(wfl);
      if (__builtin_amdgcn_ballot_w64(wfl[0] != tag[0] || wfl[1] != tag[1]) != 0) await<1>(rws, lane * 128, O_FL + (256 + 64 * w) * 128, tag);
    }
    const u32 kn = (rot + it + 3) & 15, soA = kn * 256 + w * 65536, soB = sB + kn * 163840;
    SB;
    sfor<4>([&](auto K_) {
      constexpr int K = K_.value, U = (K + 1) & 1, G = 4 * it + K - 1;
      sfor<10>([&](auto G_) {
        constexpr int g = G_.value, i = g >> 1, m = 10 * K + g;
        if constexpr (G == 0) { if constexpr (g & 1) mfB0<4 * G>(aQ[i], fr[U][1 + i]); else mf0(aP[i], fr[U][1 + i], fr[U][0]); }
        else if constexpr (G > 0) { if constexpr (g & 1) mfB<4 * G>(aQ[i], fr[U][1 + i]); else mf(aP[i], fr[U][1 + i], fr[U][0]); }
        SB;
        if constexpr (g < 6) {
          constexpr int r = g == 0 ? 1 : g == 1 ? 0 : g;
          if constexpr (r == 0) fr[U ^ 1][0] = lds16(ra[K] + (it & 3) * 16384);
          else fr[U ^ 1][r] = lds16(((it & 2) ? rb1 : rb0) + (it & 1) * 20480 + K * 5120 + (r - 1) * 256);
        }
        constexpr int n = m / 4;
        if constexpr (it < 13 && n < (it == 0 ? 5 : 9)) {
          if constexpr (m % 4 == 2) { if constexpr (n < 5) setm0<((it + 3) & 3) * 20480 + n * 1024>(mB); else setm0<((it + 3) & 3) * 16384 + (n - 5) * 1024>(mA); }
          if constexpr (m % 4 == 3) { if constexpr (n < 5) dmago<1, 0>(vb[n], rws, soB); else dmago<0, 0>(va[n - 5], rcr, soA); }
        }
        if constexpr (it == 8 && m == 39) ldv8<1, 0>(wfl, lane * 128, rws, O_FL + (256 + 64 * w) * 128);
        if constexpr (it >= 13 && n < 9 && m % 4 == 3) load_d(std::integral_constant<int, 9 * (it - 13) + n>{});
        SB;
      });
    });
  });
  sfor<5>([&](auto I_) { constexpr int i = I_.value; mf(aP[i], fr[1][1 + i], fr[1][0]); mfB<252>(aQ[i], fr[1][1 + i]); });
  mfence5(aP);
  mfence5(aQ);
  STAMP(8);
#pragma unroll
  for (int i = 0; i < 5; ++i) {
    const u32x4 o = {pk(aP[i][0], aP[i][1]), pk(aP[i][2], aP[i][3]), pk(aQ[i][0], aQ[i][1]), pk(aQ[i][2], aQ[i][3])};
    st16<SC1>(o, rws, v0 + (i & 3) * 1024, O_PQ + ((4 * rg + w) * 40 + 5 * j + (i & ~3)) * 1024);
  }
  SB;
  sfor<23>([&](auto N_) { load_d(std::integral_constant<int, 27 + N_.value>{}); });
  waitv<23>();
  STAMP(9);
  bar();
  if (w == 0) publish(rws, 2, gid, tag, lane);
  await<1>(rws, (lane & 1) * 128, O_FL + (512 + rg * 8 + 2 * w) * 128, tag);
  STAMP(10);

  const int nb0 = mi < 12 ? 7 * mi : 84 + 6 * (mi - 12), odd = mi < 12 ? (mi & 1) : 0;
  auto nbi = [&](int i) { int v = i + odd; v = v >= 7 ? 0 : v; return mi >= 12 && i == 6 ? nb0 : nb0 + v; };
  auto dma_w1 = [&](auto I_) {
    constexpr int i = I_.value;
    const u32 m = l0 + 5120 * w, s = O_W1P + (nbi(i) * 20 + 5 * w) * 1024;
    setm0<i * 20480>(m);
    asm volatile("s_nop 0");
    dmago<1, 0>(v0, rws, s); dmago<1, 1024>(v0, rws, s); dmago<1, 2048>(v0, rws, s); dmago<1, 3072>(v0, rws, s);
    dma<1, i * 20480 + 4096>(m, v0, rws, s + 4096);
  };
  const int k0 = w < 2 ? 14 * w : 28 + 13 * (w - 2);
  const u32 vlast = w < 2 ? v0 : 0x7f000000u;
  u32 sW[5];
#pragma unroll
  for (int i = 0; i < 5; ++i) sW[i] = O_W2P + (blk5(j, i) * 54 + k0) * 1024;
  auto load_w2 = [&](auto N_) {
    constexpr int n = N_.value, k = n / 5, i = n % 5;
    lda<4 * n, 1, (k & 3) * 1024>(v0, rws, sW[i] + (k >> 2) * 4096);
  };
  u32x4 wv[10];
  const u32 hb = O_H + (4 * rg + w) * 20480;
  const int kp5 = (5 * j + 1) >> 1;
  {
    u32x2 xr[5];
    f32x4 acc[5];
    ksplit<10, 40, 4, 30>(acc, wv, rws, O_PQ + (4 * rg * 40 + 10 * w) * 1024, v0, l0, w, lane, [&]() {
      sfor<5>([&](auto I_) { ldv8<0, 0>(xr[I_.value], (u32)t * 1280 + q * 8, rx, (u32)(64 * rg + 16 * w) * 1280 + blk5(j, I_.value) * 32); });
      sfor<5>(dma_w1);
    });
    STAMP(11);
    waitv<25>();
    u32x2 h[5];
    float s1 = 0.f, s2 = 0.f;
#pragma unroll
    for (int i = 0; i < 5; ++i) {
      landed(xr[i]);
      h[i][0] = pk(acc[i][0] + blo(xr[i][0]), acc[i][1] + bhi(xr[i][0]));
      h[i][1] = pk(acc[i][2] + blo(xr[i][1]), acc[i][3] + bhi(xr[i][1]));
    }
    st16<SC1>(u32x4{h[0][0], h[0][1], h[1][0], h[1][1]}, rws, v0, hb + kp5 * 1024);
    st16<SC1>(u32x4{h[2][0], h[2][1], h[3][0], h[3][1]}, rws, v0 + 1024, hb + kp5 * 1024);
    st8<SC1>(h[4], rws, v0, hb + (blk5(j, 4) >> 1) * 1024 + (j & 1) * 8);
#pragma unroll
    for (int i = 0; i < 5; ++i) {
      const float a0 = blo(h[i][0]), a1 = bhi(h[i][0]), a2 = blo(h[i][1]), a3 = bhi(h[i][1]);
      s1 += (a0 + a1) + (a2 + a3);
      s2 += a0 * a0 + a1 * a1 + a2 * a2 + a3 * a3;
    }
    s1 = addx32(addx16(s1));
    s2 = addx32(addx16(s2));
    if (lane < 16) st8<SC1>(u32x2{__float_as_uint(s1), __float_as_uint(s2)}, rws, lane * 64, O_ST + (64 * rg + 16 * w) * 64 + j * 8);
  }
  SB;
  sfor<W2_N0>(load_w2);
  waitv<W2_N0>();
  waitl0();
  STAMP(12);
  bar();
  if (w == 0) publish(rws, 3, gid, tag, lane);
  dma_w1(std::integral_constant<int, 5>{});
  dma_w1(std::integral_constant<int, 6>{});
  {
    const int tid = w * 64 + lane;
    if (tid < 112) {
      const rsrc_t rb1s = mkrs(p.b1, F * 2);
      const int i = tid >> 4;
      int v = i + odd; v = v >= 7 ? 0 : v; v = (mi >= 12 && i == 6) ? 0 : v;
      const u32 col = (u32)(nb0 + v) * 16 + (tid & 15);
      const u32x4 c = ld16<SC1>(rws, col * 16, O_CS);
      const u32 bb = __builtin_amdgcn_raw_buffer_load_b16(rb1s, col * 2, 0, 0);
      sts4(l0 + L_CS + tid * 4, (__uint_as_float(c[0]) + __uint_as_float(c[1])) + (__uint_as_float(c[2]) + __uint_as_float(c[3])));
      sts4(l0 + L_CS + 448 + tid * 4, blo(bb));
    }
  }
  await<1>(rws, (lane & 7) * 128, O_FL + (768 + sg * 16 + (w >> 1) * 8) * 128, tag);
  STAMP(13);

  {
    u32x4 a[20][2], stv[2];
    sfor<2>([&](auto M_) { ldv<1, 0>(stv[M_.value], t * 64 + q * 16, rws, O_ST + (128 * sg + 32 * w + 16 * M_.value) * 64); });
    sfor<20>([&](auto K_) {
      constexpr int k = K_.value;
      sfor<2>([&](auto M_) { ldv<1, (k & 3) * 1024>(a[k][M_.value], v0, rws, O_H + ((8 * sg + 2 * w + M_.value) * 20 + (k & ~3)) * 1024); });
    });
    waitl0();
    bar();
    STAMP(14);
    f32x4 acc[2][7];
    u32x4 wq[4];
    const u32 lb0 = l0 + v0, lb1 = lb0 + 61440, lb2 = lb0 + 122880;
    auto rdw = [&](int n) { const int k = n / 7, i = n % 7, o = (i * 20 + k) * 1024; return o < 61440 ? lds16(lb0 + o) : o < 122880 ? lds16(lb1 + (o - 61440)) : lds16(lb2 + (o - 122880)); };
#pragma unroll
    for (int n = 0; n < 3; ++n) wq[n] = rdw(n);
    SB;
    sfor<140>([&](auto N_) {
      constexpr int n = N_.value, k = n / 7, i = n % 7;
      constexpr int nw = (n + 3) / 5 < W2_N1 ? (n + 3) / 5 : W2_N1;
      if constexpr (i == 0) waitv<38 - 2 * k + nw>();
      if constexpr (k == 0) mf0(acc[0][i], wq[n & 3], a[k][0]); else mf(acc[0][i], wq[n & 3], a[k][0]);
      SB;
      if constexpr (n + 3 < 140) wq[(n + 3) & 3] = rdw(n + 3);
      SB;
      if constexpr (k == 0) mf0(acc[1][i], wq[n & 3], a[k][1]); else mf(acc[1][i], wq[n & 3], a[k][1]);
      if constexpr (n % 5 == 1 && n / 5 < W2_N1) load_w2(std::integral_constant<int, W2_N0 + n / 5>{});
      SB;
    });
    mfence7(acc[0]);
    mfence7(acc[1]);
    STAMP(15);
    float mean[2], rstd[2];
#pragma unroll
    for (int m = 0; m < 2; ++m) {
      landed(stv[m]);
      const float s1 = addx32(addx16(__uint_as_float(stv[m][0]) + __uint_as_float(stv[m][2])));
      const float s2 = addx32(addx16(__uint_as_float(stv[m][1]) + __uint_as_float(stv[m][3])));
      mean[m] = s1 * (1.f / D);
      const float var = fmaxf(s2 * (1.f / D) - mean[m] * mean[m], 0.f);
      rstd[m] = 1.f / sqrtf(var + 1e-5f);
    }
    u32x2 fo[2][7];
#pragma unroll
    for (int i = 0; i < 7; ++i) {
      const f32x4 cs = __builtin_bit_cast(f32x4, lds16(l0 + L_CS + i * 64 + q * 16)), bb = __builtin_bit_cast(f32x4, lds16(l0 + L_CS + 448 + i * 64 + q * 16));
#pragma unroll
      for (int m = 0; m < 2; ++m) {
        float v[4];
#pragma unroll
        for (int e = 0; e < 4; ++e) { const float z = (acc[m][i][e] - mean[m] * cs[e]) * rstd[m] + bb[e]; v[e] = fmaxf(z, 0.f) * z; }
        fo[m][i] = u32x2{pk(v[0], v[1]), pk(v[2], v[3])};
      }
    }
    const int kp = (nb0 + 1) >> 1;
#pragma unroll
    for (int m = 0; m < 2; ++m) {
      const u32 fb = O_F + (8 * sg + 2 * w + m) * 55296;
#pragma unroll
      for (int c = 0; c < 3; ++c) st16<SC1>(u32x4{fo[m][2 * c][0], fo[m][2 * c][1], fo[m][2 * c + 1][0], fo[m][2 * c + 1][1]}, rws, v0 + c * 1024, fb + kp * 1024);
      if (mi < 12) st8<SC1>(fo[m][6], rws, v0, fb + (nbi(6) >> 1) * 1024 + odd * 8);
    }
  }
  SB;
  constexpr int W2_N2 = 60 - W2_N0 - W2_N1;
  sfor<W2_N2>([&](auto N_) { load_w2(std::integral_constant<int, W2_N0 + W2_N1 + N_.value>{}); });
  sfor<10>([&](auto N_) { constexpr int n = N_.value; ldv<1, (n / 5) * 1024>(wv[n], n < 5 ? v0 : vlast, rws, sW[n % 5] + 3 * 4096); });
  u32x4 h01, h23;
  u32x2 h4, bv[5];
  ldv<1, 0>(h01, v0, rws, hb + kp5 * 1024);
  ldv<1, 1024>(h23, v0, rws, hb + kp5 * 1024);
  ldv8<1, 0>(h4, v0, rws, hb + (blk5(j, 4) >> 1) * 1024 + (j & 1) * 8);
  {
    const rsrc_t rb2 = mkrs(p.b2, D * 2);
    sfor<5>([&](auto I_) { ldv8<0, 0>(bv[I_.value], q * 8, rb2, blk5(j, I_.value) * 32); });
  }
  waitv<W2_N2 + 18>();
  waitl0();
  STAMP(16);
  bar();
  if (w == 0) publish(rws, 4, gid, tag, lane);
  await<1>(rws, t * 128, O_FL + (1024 + sg * 16) * 128, tag);
  STAMP(17);

  {
    f32x4 acc[5];
    ksplit<14, 54, 2, 0>(acc, wv, rws, O_F + (4 * rg * 54 + k0) * 1024, vlast, l0, w, lane, []() {});
    STAMP(18);
    landed(h01); landed(h23); landed(h4);
    const rsrc_t rout = mkrs(p.out, S * D * 2);
    const u32x2 h[5] = {{h01[0], h01[1]}, {h01[2], h01[3]}, {h23[0], h23[1]}, {h23[2], h23[3]}, h4};
#pragma unroll
    for (int i = 0; i < 5; ++i) {
      landed(bv[i]);
      u32x2 o;
      o[0] = pk(acc[i][0] + blo(bv[i][0]) + blo(h[i][0]), acc[i][1] + bhi(bv[i][0]) + bhi(h[i][0]));
      o[1] = pk(acc[i][2] + blo(bv[i][1]) + blo(h[i][1]), acc[i][3] + bhi(bv[i][1]) + bhi(h[i][1]));
      st8<0>(o, rout, (u32)t * 1280 + q * 8, (u32)(64 * rg + 16 * w) * 1280 + blk5(j, i) * 32);
    }
  }
}

extern "C" void kernel_launch(void* const* d_in, const int* in_sizes, int n_in, void* d_out, int out_size, void* d_ws, size_t ws_size,
                              hipStream_t stream) {
  Params p;
  p.x = d_in[0]; p.cr = d_in[1]; p.ci = d_in[2]; p.dr = d_in[3]; p.di = d_in[4];
  p.w1 = d_in[6]; p.b1 = d_in[7]; p.w2 = d_in[8]; p.b2 = d_in[9];
  p.out = d_out; p.ws = d_ws;
  (void)hipFuncSetAttribute((const void*)fnet_block, hipFuncAttributeMaxDynamicSharedMemorySize, LDS_BYTES);
  fnet_block<<<256, 256, LDS_BYTES, stream>>>(p);
}
```

```cpp
#define HOLD_AT 2
#define HOLD_T 1000
#include <hip/hip_runtime.h>
#include <hip/hip_bf16.h>
#include <type_traits>
#include <utility>

typedef unsigned short u16;
typedef unsigned int u32;
typedef unsigned long long u64;
typedef __attribute__((ext_vector_type(4))) u32 u32x4;
typedef __attribute__((ext_vector_type(2))) u32 u32x2;
typedef __attribute__((ext_vector_type(4))) float f32x4;
typedef __attribute__((ext_vector_type(2))) float f32x2;
typedef __attribute__((ext_vector_type(2))) __bf16 bf16x2;
typedef __attribute__((ext_vector_type(4))) short s16x4;
typedef __amdgpu_buffer_rsrc_t rsrc_t;
#define LAS __attribute__((address_space(3)))
#define DEV static __device__ __forceinline__
#define SB __builtin_amdgcn_sched_barrier(0)

constexpr int S = 2048, D = 640, F = 1728;
constexpr u32 O_XNT = 0;
constexpr u32 O_W1P = O_XNT + 2621440;
constexpr u32 O_W2P = O_W1P + 2211840;
constexpr u32 O_DP  = O_W2P + 2211840;
constexpr u32 O_PQ  = O_DP + 1638400;
constexpr u32 O_H   = O_PQ + 5242880;
constexpr u32 O_F   = O_H + 2621440;
constexpr u32 O_CS  = O_F + 7077888;
constexpr u32 O_ST  = O_CS + 27648;
constexpr u32 O_FL  = O_ST + 131072;
constexpr u32 O_NON = O_FL + 5 * 256 * 128;
constexpr u32 WS_END = O_NON + 128;
constexpr int LDS_BYTES = 163840;
constexpr int SC1 = 16;
constexpr u32 SPIN_MAX = 1u << 18;
constexpr u32 L_CR = 0, L_XB = 65536, L_LN = 147456, L_WT = L_XB + 2 * 20480;
constexpr u32 L_RED = 102400, L_CS = 143360;
#define CI_EARLY 0
#define CR_EARLY 1
#define ROT (2 * j)
#define W2_N0 20
#define W2_N1 25

#define STAMP(i) do { if ((i) == 0) t00_ = __builtin_amdgcn_s_memrealtime(); if ((i) == HOLD_AT) { while (__builtin_amdgcn_s_memrealtime() - t00_ < HOLD_T) __builtin_amdgcn_s_sleep(1); } } while (0)

struct Params {
  const void *x, *cr, *ci, *dr, *di, *w1, *b1, *w2, *b2;
  void *out, *ws;
};

template <class Fn, int... I> DEV void sfor_(Fn&& f, std::integer_sequence<int, I...>) { (f(std::integral_constant<int, I>{}), ...); }
template <int N, class Fn> DEV void sfor(Fn&& f) { sfor_(f, std::make_integer_sequence<int, N>{}); }

DEV rsrc_t mkrs(const void* p, u32 bytes) { return __builtin_amdgcn_make_buffer_rsrc((void*)p, 0, (int)bytes, 0x00020000); }
template <int AUX> DEV u32x4 ld16(rsrc_t r, u32 v, u32 s) { return __builtin_amdgcn_raw_buffer_load_b128(r, v, s, AUX); }
template <int AUX> DEV u32x2 ld8(rsrc_t r, u32 v, u32 s) { return __builtin_amdgcn_raw_buffer_load_b64(r, v, s, AUX); }
template <int AUX> DEV void st16(u32x4 d, rsrc_t r, u32 v, u32 s) { __builtin_amdgcn_raw_buffer_store_b128(d, r, v, s, AUX); }
template <int AUX> DEV void st8(u32x2 d, rsrc_t r, u32 v, u32 s) { __builtin_amdgcn_raw_buffer_store_b64(d, r, v, s, AUX); }
template <int AUX> DEV void st4(u32 d, rsrc_t r, u32 v, u32 s) { __builtin_amdgcn_raw_buffer_store_b32(d, r, v, s, AUX); }
template <int IMM> DEV void setm0(u32 base) { asm volatile("s_add_u32 m0, %0, %1" ::"s"(base), "n"(IMM) : "scc"); }
template <int SC, int IMM> DEV void dmago(u32 voff, rsrc_t r, u32 soff) {
  if constexpr (SC) asm volatile("buffer_load_dwordx4 %0, %1, %2 offen offset:%3 sc1 lds" ::"v"(voff), "s"(r), "s"(soff), "n"(IMM) : "memory");
  else asm volatile("buffer_load_dwordx4 %0, %1, %2 offen offset:%3 lds" ::"v"(voff), "s"(r), "s"(soff), "n"(IMM) : "memory");
}
template <int SC, int MI> DEV void dma(u32 m0, u32 voff, rsrc_t r, u32 soff) {
  setm0<MI>(m0);
  asm volatile("s_nop 0");
  dmago<SC, 0>(voff, r, soff);
}
template <int R, int SC, int IMM> DEV void lda(u32 voff, rsrc_t r, u32 soff) {
  if constexpr (SC) asm volatile("buffer_load_dwordx4 a[%0:%1], %2, %3, %4 offen offset:%5 sc1" ::"n"(R), "n"(R + 3), "v"(voff), "s"(r), "s"(soff), "n"(IMM) : "memory");
  else asm volatile("buffer_load_dwordx4 a[%0:%1], %2, %3, %4 offen offset:%5" ::"n"(R), "n"(R + 3), "v"(voff), "s"(r), "s"(soff), "n"(IMM) : "memory");
}
template <int SC, int IMM> DEV void ldv(u32x4& d, u32 voff, rsrc_t r, u32 soff) {
  if constexpr (SC) asm volatile("buffer_load_dwordx4 %0, %1, %2, %3 offen offset:%4 sc1" : "=v"(d) : "v"(voff), "s"(r), "s"(soff), "n"(IMM) : "memory");
  else asm volatile("buffer_load_dwordx4 %0, %1, %2, %3 offen offset:%4" : "=v"(d) : "v"(voff), "s"(r), "s"(soff), "n"(IMM) : "memory");
}
template <int SC, int IMM> DEV void ldv8(u32x2& d, u32 voff, rsrc_t r, u32 soff) {
  if constexpr (SC) asm volatile("buffer_load_dwordx2 %0, %1, %2, %3 offen offset:%4 sc1" : "=v"(d) : "v"(voff), "s"(r), "s"(soff), "n"(IMM) : "memory");
  else asm volatile("buffer_load_dwordx2 %0, %1, %2, %3 offen offset:%4" : "=v"(d) : "v"(voff), "s"(r), "s"(soff), "n"(IMM) : "memory");
}
template <int N> DEV void waitv() { asm volatile("s_waitcnt vmcnt(%0)" ::"n"(N > 63 ? 63 : N) : "memory"); }
template <class T> DEV void landed(T& v) { asm volatile("; landed %0" : "+v"(v)); }
DEV void waitl0() { asm volatile("s_waitcnt lgkmcnt(0)" ::: "memory"); }
DEV void bar() { asm volatile("s_barrier" ::: "memory"); }
DEV void mf(f32x4& c, const u32x4& a, const u32x4& b) { asm volatile("v_mfma_f32_16x16x32_bf16 %0, %1, %2, %0" : "+v"(c) : "v"(a), "v"(b)); }
DEV void mf0(f32x4& c, const u32x4& a, const u32x4& b) { asm volatile("v_mfma_f32_16x16x32_bf16 %0, %1, %2, 0" : "=&v"(c) : "v"(a), "v"(b)); }
template <int R> DEV void mfB(f32x4& c, const u32x4& a) { asm volatile("v_mfma_f32_16x16x32_bf16 %0, %1, a[%2:%3], %0" : "+v"(c) : "v"(a), "n"(R), "n"(R + 3)); }
template <int R> DEV void mfB0(f32x4& c, const u32x4& a) { asm volatile("v_mfma_f32_16x16x32_bf16 %0, %1, a[%2:%3], 0" : "=&v"(c) : "v"(a), "n"(R), "n"(R + 3)); }
template <int R> DEV void mfA(f32x4& c, const u32x4& b) { asm volatile("v_mfma_f32_16x16x32_bf16 %0, a[%2:%3], %1, %0" : "+v"(c) : "v"(b), "n"(R), "n"(R + 3)); }
template <int R> DEV void mfA0(f32x4& c, const u32x4& b) { asm volatile("v_mfma_f32_16x16x32_bf16 %0, a[%2:%3], %1, 0" : "=&v"(c) : "v"(b), "n"(R), "n"(R + 3)); }
DEV void mfence5(f32x4 (&c)[5]) { asm volatile("s_nop 7" : "+v"(c[0]), "+v"(c[1]), "+v"(c[2]), "+v"(c[3]), "+v"(c[4])); }
DEV void mfence7(f32x4 (&c)[7]) { asm volatile("s_nop 7" : "+v"(c[0]), "+v"(c[1]), "+v"(c[2]), "+v"(c[3]), "+v"(c[4]), "+v"(c[5]), "+v"(c[6])); }

DEV u32x4 lds16(u32 a) { return *(const LAS u32x4*)(size_t)a; }
DEV u32x2 lds8(u32 a) { return *(const LAS u32x2*)(size_t)a; }
DEV void sts16(u32 a, u32x4 v) { *(LAS u32x4*)(size_t)a = v; }
DEV void sts4(u32 a, float v) { *(LAS float*)(size_t)a = v; }
DEV u32x4 tr16(u32 lo, u32 hi) {
  const u32x2 a = __builtin_bit_cast(u32x2, __builtin_amdgcn_ds_read_tr16_b64_v4i16((LAS s16x4*)(size_t)lo));
  const u32x2 b = __builtin_bit_cast(u32x2, __builtin_amdgcn_ds_read_tr16_b64_v4i16((LAS s16x4*)(size_t)hi));
  return u32x4{a[0], a[1], b[0], b[1]};
}
DEV u32 pk(float lo, float hi) { f32x2 v = {lo, hi}; return __builtin_bit_cast(u32, __builtin_convertvector(v, bf16x2)); }
DEV float blo(u32 u) { return __uint_as_float(u << 16); }
DEV float bhi(u32 u) { return __uint_as_float(u & 0xffff0000u); }
template <int CTRL> DEV float dppadd(float v) {
  return v + __uint_as_float(__builtin_amdgcn_update_dpp(0u, __float_as_uint(v), CTRL, 0xf, 0xf, true));
}
DEV float sum16(float v) { v = dppadd<0x128>(v); v = dppadd<0x124>(v); v = dppadd<0x122>(v); v = dppadd<0x121>(v); return v; }
DEV float addx16(float v) { u32 u = __float_as_uint(v); auto r = __builtin_amdgcn_permlane16_swap(u, u, false, false); return __uint_as_float(r[0]) + __uint_as_float(r[1]); }
DEV float addx32(float v) { u32 u = __float_as_uint(v); auto r = __builtin_amdgcn_permlane32_swap(u, u, false, false); return __uint_as_float(r[0]) + __uint_as_float(r[1]); }
DEV int blk5(int j, int i) { int v = i + (j & 1); return 5 * j + (v >= 5 ? v - 5 : v); }

DEV void publish(rsrc_t rws, int edge, int g, u32x2 tag, int lane) {
  if (lane == 0) st8<SC1>(tag, rws, 0, O_FL + (edge * 256 + g) * 128);
}
template <int NL> DEV void await(rsrc_t rws, u32 voff, u32 soff, u32x2 tag) {
  for (u32 spin = 0; spin < SPIN_MAX; ++spin) {
    u32x2 v[NL];
    sfor<NL>([&](auto C_) { ldv8<1, 0>(v[C_.value], voff, rws, soff + C_.value * 8192); });
    waitv<0>();
    bool ok = true;
#pragma unroll
    for (int c = 0; c < NL; ++c) { landed(v[c]); ok &= (v[c][0] == tag[0]) & (v[c][1] == tag[1]); }
    if (__builtin_amdgcn_ballot_w64(!ok) == 0) break;
  }
}

template <int NK, int PITCH, int DEPTH, int NB, class Mid>
DEV void ksplit(f32x4 (&acc)[5], const u32x4 (&wv)[10], rsrc_t rws, u32 sA, u32 vlast, u32 l0, int w, int lane, Mid&& mid) {
  u32x4 a[DEPTH][NK];
  const u32 v0 = lane * 16;
  auto loadA = [&](auto S_) {
    constexpr int s = S_.value;
    const u32 base = sA + ((w + 1 + s) & 3) * (PITCH * 1024);
    sfor<NK>([&](auto K_) { constexpr int k = K_.value; ldv<1, (k & 3) * 1024>(a[s % DEPTH][k], k == 13 ? vlast : v0, rws, base + (k >> 2) * 4096); });
  };
  sfor<DEPTH>(loadA);
  mid();
  sfor<4>([&](auto S_) {
    constexpr int s = S_.value;
    sfor<NK>([&](auto K_) {
      constexpr int k = K_.value;
      constexpr int issued = (s + DEPTH < 4 ? s + DEPTH : 4) * NK;
      waitv<issued - (s * NK + k + 1) + (s < DEPTH ? NB : 0)>();
      sfor<5>([&](auto I_) {
        constexpr int i = I_.value;
        if constexpr (k == 0) mfA0<4 * (5 * k + i)>(acc[i], a[s % DEPTH][k]);
        else if constexpr (k < 12) mfA<4 * (5 * k + i)>(acc[i], a[s % DEPTH][k]);
        else mf(acc[i], wv[5 * (k - 12) + i], a[s % DEPTH][k]);
      });
    });
    if constexpr (s + DEPTH < 4) loadA(std::integral_constant<int, s + DEPTH>{});
    mfence5(acc);
    if constexpr (s < 3) {
      const u32 dst = l0 + L_RED + (((w + 1 + s) & 3) * 3 + s) * 5120 + v0;
#pragma unroll
      for (int i = 0; i < 5; ++i) sts16(dst + i * 1024, __builtin_bit_cast(u32x4, acc[i]));
    }
    SB;
  });
  waitl0();
  bar();
  const u32 src = l0 + L_RED + w * 15360 + v0;
#pragma unroll
  for (int s = 0; s < 3; ++s)
#pragma unroll
    for (int i = 0; i < 5; ++i) acc[i] += __builtin_bit_cast(f32x4, lds16(src + s * 5120 + i * 1024));
}

__global__ __launch_bounds__(256, 1) void fnet_block(Params p) {
  extern __shared__ __attribute__((aligned(16))) char smem[];
  asm volatile("" ::: "a0", "a63", "a127", "a191", "a255");
  const int b = blockIdx.x, w = __builtin_amdgcn_readfirstlane(threadIdx.x >> 6), lane = threadIdx.x & 63;
  const int l = b >> 3, sg = (b & 7) * 2 + (l >> 4), mi = l & 15, rg = sg * 2 + (mi >> 3), j = mi & 7, gid = sg * 16 + mi;
  const int t = lane & 15, q = lane >> 4;
  const u32 l0 = (u32)(size_t)(const LAS char*)smem, v0 = lane * 16;
  const rsrc_t rws = mkrs(p.ws, WS_END), rx = mkrs(p.x, S * D * 2), rcr = mkrs(p.cr, S * S * 2), rci = mkrs(p.ci, S * S * 2);
  u64 t00_;
  STAMP(0);
  u64 non;
  asm volatile("s_load_dwordx2 %0, %1, %2" : "=s"(non) : "s"(p.ws), "s"(O_NON) : "memory");
  const int rot = ROT;
  u32 va[4], vb[5];
#pragma unroll
  for (int i = 0; i < 4; ++i) { const u32 row = 4 * i + q; va[i] = (u32)(64 * rg + row) * 4096 + ((t ^ row) << 4); }
  const u32 vci = (u32)(64 * rg + 16 * w + t) * 4096 + q * 16;
  auto load_ci = [&](auto IT_) {
    constexpr int it = IT_.value;
    const u32 so = ((rot + it) & 15) * 256;
    sfor<4>([&](auto K_) { constexpr int k = K_.value; lda<16 * it + 4 * k, 0, 64 * k>(vci, rci, so); });
  };
  auto dma_cr = [&](auto IT_, int ww) {
    constexpr int it = IT_.value;
    const u32 so = ((rot + it) & 15) * 256 + ww * 65536, m = l0 + L_CR + ww * 4096;
    sfor<4>([&](auto N_) { constexpr int n = N_.value; dma<0, (it & 3) * 16384 + n * 1024>(m, va[n], rcr, so); });
  };

  const int hw = w - 2;
  const u32 lwt = l0 + L_WT + hw * 20480;
  if (w < 2) {
    u32x4 xv[5];
    const u32 v = (u32)(8 * b + 4 * w + q) * 1280 + t * 16;
#pragma unroll
    for (int c = 0; c < 5; ++c) xv[c] = ld16<0>(rx, v + c * 256, 0);
    float f[40], s = 0.f;
#pragma unroll
    for (int c = 0; c < 5; ++c)
#pragma unroll
      for (int e = 0; e < 4; ++e) { f[8 * c + 2 * e] = blo(xv[c][e]); f[8 * c + 2 * e + 1] = bhi(xv[c][e]); }
#pragma unroll
    for (int c = 0; c < 40; ++c) s += f[c];
    const float mean = sum16(s) * (1.f / D);
    float q2 = 0.f;
#pragma unroll
    for (int c = 0; c < 40; ++c) { f[c] -= mean; q2 += f[c] * f[c]; }
    const float rs = 1.f / sqrtf(sum16(q2) * (1.f / D) + 1e-5f);
    const u32 dst = l0 + L_LN + (4 * w + q) * 1280 + t * 16;
#pragma unroll
    for (int c = 0; c < 5; ++c)
      sts16(dst + c * 256, u32x4{pk(f[8 * c] * rs, f[8 * c + 1] * rs), pk(f[8 * c + 2] * rs, f[8 * c + 3] * rs), pk(f[8 * c + 4] * rs, f[8 * c + 5] * rs), pk(f[8 * c + 6] * rs, f[8 * c + 7] * rs)});
    waitl0();
  } else {
    if (b < 108) {
      const rsrc_t r = mkrs(p.w1, D * F * 2);
      const int cg = b % 27, kq = b / 27;
      const u32 v = (u32)(lane >> 2) * 3456 + (lane & 3) * 16, s0 = (u32)(160 * kq) * 3456 + cg * 128 + hw * 64;
      sfor<10>([&](auto I_) { constexpr int i = I_.value; dma<0, i * 1024>(lwt, v, r, s0 + i * 16 * 3456); });
    } else if (b < 216) {
      const rsrc_t r = mkrs(p.w2, D * F * 2);
      const int u = b - 108, ks = u >> 1, ch = u & 1;
      const u32 s0 = (u32)ks * 32 * 1280 + ch * 640 + hw * 320;
      sfor<10>([&](auto I_) {
        constexpr int i = I_.value;
        const u32 o = i * 64 + lane, row = o / 20;
        dma<0, i * 1024>(lwt, row * 1280 + (o - row * 20) * 16, r, s0);
      });
    } else {
      const rsrc_t r0 = mkrs(p.dr, D * D * 2), r1 = mkrs(p.di, D * D * 2);
      const u32 s0 = (u32)(b - 216) * 20480 + hw * 640;
      sfor<10>([&](auto I_) {
        constexpr int i = I_.value;
        const u32 o = i * 64 + lane, row = o / 40, c = o - row * 40;
        const u32 v = row * 1280 + ((c ^ ((row >> 1) & 7)) << 4);
        dma<0, i * 1024>(lwt, v, r0, s0);
        dma<0, 10240 + i * 1024>(lwt, v, r1, s0);
      });
    }
    if (CR_EARLY) sfor<4>([&](auto IT_) { dma_cr(IT_, hw); dma_cr(IT_, w); });
    sfor<CI_EARLY>(load_ci);
  }
  asm volatile("s_waitcnt lgkmcnt(0)" : "+s"(non)::"memory");
  non += 0x9E3779B97F4A7C15ull;
  const u32x2 tag = {(u32)non, (u32)(non >> 32)};
  STAMP(1);
  bar();
  STAMP(2);
  if (w == 0) {
    const u32 a = l0 + L_LN + (t >> 2) * 1280 + q * 32 + (t & 3) * 8;
#pragma unroll
    for (int c = 0; c < 10; ++c) st16<SC1>(tr16(a + c * 128, a + c * 128 + 4 * 1280), rws, v0 + (c & 3) * 1024, O_XNT + b * 10240 + (c >> 2) * 4096);
    waitv<0>();
    publish(rws, 0, b, tag, lane);
    STAMP(3);
  } else if (w >= 2) {
    waitv<(CR_EARLY ? 32 : 0) + 4 * CI_EARLY>();
    if (b < 108) {
      const int cg = b % 27, kq = b / 27;
      const u32 a = lwt + (4 * q + (t >> 2)) * 64 + (t & 3) * 8;
#pragma unroll
      for (int n = 0; n < 2; ++n) {
        float cs = 0.f;
#pragma unroll
        for (int i = 0; i < 5; ++i) {
          const u32x4 o = tr16(a + n * 32 + i * 2048, a + n * 32 + i * 2048 + 1024);
          st16<SC1>(o, rws, v0, O_W1P + ((cg * 4 + hw * 2 + n) * 20 + kq * 5 + i) * 1024);
#pragma unroll
          for (int e = 0; e < 4; ++e) cs += blo(o[e]) + bhi(o[e]);
        }
        cs = addx32(addx16(cs));
        if (lane < 16) st4<SC1>(__float_as_uint(cs), rws, v0, O_CS + (cg * 64 + hw * 32 + n * 16) * 16 + kq * 4);
      }
    } else if (b < 216) {
      const int u = b - 108, ks = u >> 1, ch = u & 1;
      const u32 a = lwt + (4 * q + (t >> 2)) * 320 + (t & 3) * 8;
#pragma unroll
      for (int i = 0; i < 10; ++i) st16<SC1>(tr16(a + i * 32, a + i * 32 + 16 * 320), rws, v0, O_W2P + ((ch * 20 + hw * 10 + i) * 54 + ks) * 1024);
    } else {
#pragma unroll
      for (int i = 0; i < 20; ++i) {
        const u32 c = 2 * i + (q >> 1);
        const u32 a = lwt + t * 640 + ((c ^ (t >> 1)) << 4) + (q & 1) * 8;
        const u32x2 re = lds8(a), im = lds8(a + 10240);
        st16<SC1>(u32x4{re[0], re[1], im[0] ^ 0x80008000u, im[1] ^ 0x80008000u}, rws, v0 + (i & 3) * 1024, O_DP + ((b - 216) * 40 + hw * 20 + (i & ~3)) * 1024);
      }
    }
    waitl0();
  }
  if (!CR_EARLY) sfor<4>([&](auto IT_) { dma_cr(IT_, w); });
  if (CI_EARLY > 0 && w < 2) sfor<CI_EARLY>(load_ci);
  sfor<16 - CI_EARLY>([&](auto I_) { load_ci(std::integral_constant<int, I_.value + CI_EARLY>{}); });
  STAMP(4);
  if (w == 3) await<4>(rws, lane * 128, O_FL, tag);
  STAMP(5);
  bar();
  STAMP(6);

#pragma unroll
  for (int i = 0; i < 5; ++i) { const u32 o = (u32)(w * 5 + i) * 64 + lane, sb = o / 80; vb[i] = sb * 10240 + (o - sb * 80) * 16; }
  const u32 mB = l0 + L_XB + w * 5120, mA = l0 + L_CR + w * 4096, sB = O_XNT + j * 1280;
  sfor<3>([&](auto IT_) {
    constexpr int it = IT_.value;
    const u32 so = sB + ((rot + it) & 15) * 163840;
    sfor<5>([&](auto N_) { constexpr int n = N_.value; dma<1, it * 20480 + n * 1024>(mB, vb[n], rws, so); });
  });
  f32x4 aP[5], aQ[5];
  u32x4 fr[2][6];
  u32x2 wfl;
  u32 ra[4];
#pragma unroll
  for (int k = 0; k < 4; ++k) ra[k] = l0 + L_CR + w * 4096 + t * 256 + (((4 * k + q) ^ t) << 4);
  const u32 rb0 = l0 + L_XB + q * 1280 + t * 16, rb1 = rb0 + 40960;
  u32 sD[5];
#pragma unroll
  for (int i = 0; i < 5; ++i) sD[i] = O_DP + (blk5(j, i) * 40 + 10 * w) * 1024;
  auto load_d = [&](auto N_) {
    constexpr int n = N_.value, k = n / 5, i = n % 5;
    lda<4 * n, 1, (k & 3) * 1024>(v0, rws, sD[i] + (k >> 2) * 4096);
  };
  auto cnt = [](int it) { return it < 0 ? 5 : it == 0 ? 5 : it == 8 ? 10 : 9; };
  sfor<16>([&](auto IT_) {
    constexpr int it = IT_.value;
    waitv<cnt(it - 2) + cnt(it - 1)>();
    waitl0();
    bar();
    if constexpr (it == 0) {
      STAMP(7);
      if (w == 0) { publish(rws, 1, b, tag, lane); if (b == 0 && lane == 0) st8<SC1>(tag, rws, 0, O_NON); }
    }
    if constexpr (it == 11) {
      landed(wfl);
      if (__builtin_amdgcn_ballot_w64(wfl[0] != tag[0] || wfl[1] != tag[1]) != 0) await<1>(rws, lane * 128, O_FL + (256 + 64 * w) * 128, tag);
    }
    const u32 kn = (rot + it + 3) & 15, soA = kn * 256 + w * 65536, soB = sB + kn * 163840;
    SB;
    sfor<4>([&](auto K_) {
      constexpr int K = K_.value, U = (K + 1) & 1, G = 4 * it + K - 1;
      sfor<10>([&](auto G_) {
        constexpr int g = G_.value, i = g >> 1, m = 10 * K + g;
        if constexpr (G == 0) { if constexpr (g & 1) mfB0<4 * G>(aQ[i], fr[U][1 + i]); else mf0(aP[i], fr[U][1 + i], fr[U][0]); }
        else if constexpr (G > 0) { if constexpr (g & 1) mfB<4 * G>(aQ[i], fr[U][1 + i]); else mf(aP[i], fr[U][1 + i], fr[U][0]); }
        SB;
        if constexpr (g < 6) {
          constexpr int r = g == 0 ? 1 : g == 1 ? 0 : g;
          if constexpr (r == 0) fr[U ^ 1][0] = lds16(ra[K] + (it & 3) * 16384);
          else fr[U ^ 1][r] = lds16(((it & 2) ? rb1 : rb0) + (it & 1) * 20480 + K * 5120 + (r - 1) * 256);
        }
        constexpr int n = m / 4;
        if constexpr (it < 13 && n < (it == 0 ? 5 : 9)) {
          if constexpr (m % 4 == 2) { if constexpr (n < 5) setm0<((it + 3) & 3) * 20480 + n * 1024>(mB); else setm0<((it + 3) & 3) * 16384 + (n - 5) * 1024>(mA); }
          if constexpr (m % 4 == 3) { if constexpr (n < 5) dmago<1, 0>(vb[n], rws, soB); else dmago<0, 0>(va[n - 5], rcr, soA); }
        }
        if constexpr (it == 8 && m == 39) ldv8<1, 0>(wfl, lane * 128, rws, O_FL + (256 + 64 * w) * 128);
        if constexpr (it >= 13 && n < 9 && m % 4 == 3) load_d(std::integral_constant<int, 9 * (it - 13) + n>{});
        SB;
      });
    });
  });
  sfor<5>([&](auto I_) { constexpr int i = I_.value; mf(aP[i], fr[1][1 + i], fr[1][0]); mfB<252>(aQ[i], fr[1][1 + i]); });
  mfence5(aP);
  mfence5(aQ);
  STAMP(8);
#pragma unroll
  for (int i = 0; i < 5; ++i) {
    const u32x4 o = {pk(aP[i][0], aP[i][1]), pk(aP[i][2], aP[i][3]), pk(aQ[i][0], aQ[i][1]), pk(aQ[i][2], aQ[i][3])};
    st16<SC1>(o, rws, v0 + (i & 3) * 1024, O_PQ + ((4 * rg + w) * 40 + 5 * j + (i & ~3)) * 1024);
  }
  SB;
  sfor<23>([&](auto N_) { load_d(std::integral_constant<int, 27 + N_.value>{}); });
  waitv<23>();
  STAMP(9);
  bar();
  if (w == 0) publish(rws, 2, gid, tag, lane);
  await<1>(rws, (lane & 1) * 128, O_FL + (512 + rg * 8 + 2 * w) * 128, tag);
  STAMP(10);

  const int nb0 = mi < 12 ? 7 * mi : 84 + 6 * (mi - 12), odd = mi < 12 ? (mi & 1) : 0;
  auto nbi = [&](int i) { int v = i + odd; v = v >= 7 ? 0 : v; return mi >= 12 && i == 6 ? nb0 : nb0 + v; };
  auto dma_w1 = [&](auto I_) {
    constexpr int i = I_.value;
    const u32 m = l0 + 5120 * w, s = O_W1P + (nbi(i) * 20 + 5 * w) * 1024;
    setm0<i * 20480>(m);
    asm volatile("s_nop 0");
    dmago<1, 0>(v0, rws, s); dmago<1, 1024>(v0, rws, s); dmago<1, 2048>(v0, rws, s); dmago<1, 3072>(v0, rws, s);
    dma<1, i * 20480 + 4096>(m, v0, rws, s + 4096);
  };
  const int k0 = w < 2 ? 14 * w : 28 + 13 * (w - 2);
  const u32 vlast = w < 2 ? v0 : 0x7f000000u;
  u32 sW[5];
#pragma unroll
  for (int i = 0; i < 5; ++i) sW[i] = O_W2P + (blk5(j, i) * 54 + k0) * 1024;
  auto load_w2 = [&](auto N_) {
    constexpr int n = N_.value, k = n / 5, i = n % 5;
    lda<4 * n, 1, (k & 3) * 1024>(v0, rws, sW[i] + (k >> 2) * 4096);
  };
  u32x4 wv[10];
  const u32 hb = O_H + (4 * rg + w) * 20480;
  const int kp5 = (5 * j + 1) >> 1;
  {
    u32x2 xr[5];
    f32x4 acc[5];
    ksplit<10, 40, 4, 30>(acc, wv, rws, O_PQ + (4 * rg * 40 + 10 * w) * 1024, v0, l0, w, lane, [&]() {
      sfor<5>([&](auto I_) { ldv8<0, 0>(xr[I_.value], (u32)t * 1280 + q * 8, rx, (u32)(64 * rg + 16 * w) * 1280 + blk5(j, I_.value) * 32); });
      sfor<5>(dma_w1);
    });
    STAMP(11);
    waitv<25>();
    u32x2 h[5];
    float s1 = 0.f, s2 = 0.f;
#pragma unroll
    for (int i = 0; i < 5; ++i) {
      landed(xr[i]);
      h[i][0] = pk(acc[i][0] + blo(xr[i][0]), acc[i][1] + bhi(xr[i][0]));
      h[i][1] = pk(acc[i][2] + blo(xr[i][1]), acc[i][3] + bhi(xr[i][1]));
    }
    st16<SC1>(u32x4{h[0][0], h[0][1], h[1][0], h[1][1]}, rws, v0, hb + kp5 * 1024);
    st16<SC1>(u32x4{h[2][0], h[2][1], h[3][0], h[3][1]}, rws, v0 + 1024, hb + kp5 * 1024);
    st8<SC1>(h[4], rws, v0, hb + (blk5(j, 4) >> 1) * 1024 + (j & 1) * 8);
#pragma unroll
    for (int i = 0; i < 5; ++i) {
      const float a0 = blo(h[i][0]), a1 = bhi(h[i][0]), a2 = blo(h[i][1]), a3 = bhi(h[i][1]);
      s1 += (a0 + a1) + (a2 + a3);
      s2 += a0 * a0 + a1 * a1 + a2 * a2 + a3 * a3;
    }
    s1 = addx32(addx16(s1));
    s2 = addx32(addx16(s2));
    if (lane < 16) st8<SC1>(u32x2{__float_as_uint(s1), __float_as_uint(s2)}, rws, lane * 64, O_ST + (64 * rg + 16 * w) * 64 + j * 8);
  }
  SB;
  sfor<W2_N0>(load_w2);
  waitv<W2_N0>();
  waitl0();
  STAMP(12);
  bar();
  if (w == 0) publish(rws, 3, gid, tag, lane);
  dma_w1(std::integral_constant<int, 5>{});
  dma_w1(std::integral_constant<int, 6>{});
  {
    const int tid = w * 64 + lane;
    if (tid < 112) {
      const rsrc_t rb1s = mkrs(p.b1, F * 2);
      const int i = tid >> 4;
      int v = i + odd; v = v >= 7 ? 0 : v; v = (mi >= 12 && i == 6) ? 0 : v;
      const u32 col = (u32)(nb0 + v) * 16 + (tid & 15);
      const u32x4 c = ld16<SC1>(rws, col * 16, O_CS);
      const u32 bb = __builtin_amdgcn_raw_buffer_load_b16(rb1s, col * 2, 0, 0);
      sts4(l0 + L_CS + tid * 4, (__uint_as_float(c[0]) + __uint_as_float(c[1])) + (__uint_as_float(c[2]) + __uint_as_float(c[3])));
      sts4(l0 + L_CS + 448 + tid * 4, blo(bb));
    }
  }
  await<1>(rws, (lane & 7) * 128, O_FL + (768 + sg * 16 + (w >> 1) * 8) * 128, tag);
  STAMP(13);

  {
    u32x4 a[20][2], stv[2];
    sfor<2>([&](auto M_) { ldv<1, 0>(stv[M_.value], t * 64 + q * 16, rws, O_ST + (128 * sg + 32 * w + 16 * M_.value) * 64); });
    sfor<20>([&](auto K_) {
      constexpr int k = K_.value;
      sfor<2>([&](auto M_) { ldv<1, (k & 3) * 1024>(a[k][M_.value], v0, rws, O_H + ((8 * sg + 2 * w + M_.value) * 20 + (k & ~3)) * 1024); });
    });
    waitl0();
    bar();
    STAMP(14);
    f32x4 acc[2][7];
    u32x4 wq[4];
    const u32 lb0 = l0 + v0, lb1 = lb0 + 61440, lb2 = lb0 + 122880;
    auto rdw = [&](int n) { const int k = n / 7, i = n % 7, o = (i * 20 + k) * 1024; return o < 61440 ? lds16(lb0 + o) : o < 122880 ? lds16(lb1 + (o - 61440)) : lds16(lb2 + (o - 122880)); };
#pragma unroll
    for (int n = 0; n < 3; ++n) wq[n] = rdw(n);
    SB;
    sfor<140>([&](auto N_) {
      constexpr int n = N_.value, k = n / 7, i = n % 7;
      constexpr int nw = (n + 3) / 5 < W2_N1 ? (n + 3) / 5 : W2_N1;
      if constexpr (i == 0) waitv<38 - 2 * k + nw>();
      if constexpr (k == 0) mf0(acc[0][i], wq[n & 3], a[k][0]); else mf(acc[0][i], wq[n & 3], a[k][0]);
      SB;
      if constexpr (n + 3 < 140) wq[(n + 3) & 3] = rdw(n + 3);
      SB;
      if constexpr (k == 0) mf0(acc[1][i], wq[n & 3], a[k][1]); else mf(acc[1][i], wq[n & 3], a[k][1]);
      if constexpr (n % 5 == 1 && n / 5 < W2_N1) load_w2(std::integral_constant<int, W2_N0 + n / 5>{});
      SB;
    });
    mfence7(acc[0]);
    mfence7(acc[1]);
    STAMP(15);
    float mean[2], rstd[2];
#pragma unroll
    for (int m = 0; m < 2; ++m) {
      landed(stv[m]);
      const float s1 = addx32(addx16(__uint_as_float(stv[m][0]) + __uint_as_float(stv[m][2])));
      const float s2 = addx32(addx16(__uint_as_float(stv[m][1]) + __uint_as_float(stv[m][3])));
      mean[m] = s1 * (1.f / D);
      const float var = fmaxf(s2 * (1.f / D) - mean[m] * mean[m], 0.f);
      rstd[m] = 1.f / sqrtf(var + 1e-5f);
    }
    u32x2 fo[2][7];
#pragma unroll
    for (int i = 0; i < 7; ++i) {
      const f32x4 cs = __builtin_bit_cast(f32x4, lds16(l0 + L_CS + i * 64 + q * 16)), bb = __builtin_bit_cast(f32x4, lds16(l0 + L_CS + 448 + i * 64 + q * 16));
#pragma unroll
      for (int m = 0; m < 2; ++m) {
        float v[4];
#pragma unroll
        for (int e = 0; e < 4; ++e) { const float z = (acc[m][i][e] - mean[m] * cs[e]) * rstd[m] + bb[e]; v[e] = fmaxf(z, 0.f) * z; }
        fo[m][i] = u32x2{pk(v[0], v[1]), pk(v[2], v[3])};
      }
    }
    const int kp = (nb0 + 1) >> 1;
#pragma unroll
    for (int m = 0; m < 2; ++m) {
      const u32 fb = O_F + (8 * sg + 2 * w + m) * 55296;
#pragma unroll
      for (int c = 0; c < 3; ++c) st16<SC1>(u32x4{fo[m][2 * c][0], fo[m][2 * c][1], fo[m][2 * c + 1][0], fo[m][2 * c + 1][1]}, rws, v0 + c * 1024, fb + kp * 1024);
      if (mi < 12) st8<SC1>(fo[m][6], rws, v0, fb + (nbi(6) >> 1) * 1024 + odd * 8);
    }
  }
  SB;
  constexpr int W2_N2 = 60 - W2_N0 - W2_N1;
  sfor<W2_N2>([&](auto N_) { load_w2(std::integral_constant<int, W2_N0 + W2_N1 + N_.value>{}); });
  sfor<10>([&](auto N_) { constexpr int n = N_.value; ldv<1, (n / 5) * 1024>(wv[n], n < 5 ? v0 : vlast, rws, sW[n % 5] + 3 * 4096); });
  u32x4 h01, h23;
  u32x2 h4, bv[5];
  ldv<1, 0>(h01, v0, rws, hb + kp5 * 1024);
  ldv<1, 1024>(h23, v0, rws, hb + kp5 * 1024);
  ldv8<1, 0>(h4, v0, rws, hb + (blk5(j, 4) >> 1) * 1024 + (j & 1) * 8);
  {
    const rsrc_t rb2 = mkrs(p.b2, D * 2);
    sfor<5>([&](auto I_) { ldv8<0, 0>(bv[I_.value], q * 8, rb2, blk5(j, I_.value) * 32); });
  }
  waitv<W2_N2 + 18>();
  waitl0();
  STAMP(16);
  bar();
  if (w == 0) publish(rws, 4, gid, tag, lane);
  await<1>(rws, t * 128, O_FL + (1024 + sg * 16) * 128, tag);
  STAMP(17);

  {
    f32x4 acc[5];
    ksplit<14, 54, 2, 0>(acc, wv, rws, O_F + (4 * rg * 54 + k0) * 1024, vlast, l0, w, lane, []() {});
    STAMP(18);
    landed(h01); landed(h23); landed(h4);
    const rsrc_t rout = mkrs(p.out, S * D * 2);
    const u32x2 h[5] = {{h01[0], h01[1]}, {h01[2], h01[3]}, {h23[0], h23[1]}, {h23[2], h23[3]}, h4};
#pragma unroll
    for (int i = 0; i < 5; ++i) {
      landed(bv[i]);
      u32x2 o;
      o[0] = pk(acc[i][0] + blo(bv[i][0]) + blo(h[i][0]), acc[i][1] + bhi(bv[i][0]) + bhi(h[i][0]));
      o[1] = pk(acc[i][2] + blo(bv[i][1]) + blo(h[i][1]), acc[i][3] + bhi(bv[i][1]) + bhi(h[i][1]));
      st8<0>(o, rout, (u32)t * 1280 + q * 8, (u32)(64 * rg + 16 * w) * 1280 + blk5(j, i) * 32);
    }
  }
  STAMP(19);
}

extern "C" void kernel_launch(void* const* d_in, const int* in_sizes, int n_in, void* d_out, int out_size, void* d_ws, size_t ws_size,
                              hipStream_t stream) {
  Params p;
  p.x = d_in[0]; p.cr = d_in[1]; p.ci = d_in[2]; p.dr = d_in[3]; p.di = d_in[4];
  p.w1 = d_in[6]; p.b1 = d_in[7]; p.w2 = d_in[8]; p.b2 = d_in[9];
  p.out = d_out; p.ws = d_ws;
  (void)hipFuncSetAttribute((const void*)fnet_block, hipFuncAttributeMaxDynamicSharedMemorySize, LDS_BYTES);
  fnet_block<<<256, 256, LDS_BYTES, stream>>>(p);
}
```

```cpp
#define HOLD_AT 19
#define HOLD_T 6000
#include <hip/hip_runtime.h>
#include <hip/hip_bf16.h>
#include <type_traits>
#include <utility>

typedef unsigned short u16;
typedef unsigned int u32;
typedef unsigned long long u64;
typedef __attribute__((ext_vector_type(4))) u32 u32x4;
typedef __attribute__((ext_vector_type(2))) u32 u32x2;
typedef __attribute__((ext_vector_type(4))) float f32x4;
typedef __attribute__((ext_vector_type(2))) float f32x2;
typedef __attribute__((ext_vector_type(2))) __bf16 bf16x2;
typedef __attribute__((ext_vector_type(4))) short s16x4;
typedef __amdgpu_buffer_rsrc_t rsrc_t;
#define LAS __attribute__((address_space(3)))
#define DEV static __device__ __forceinline__
#define SB __builtin_amdgcn_sched_barrier(0)

constexpr int S = 2048, D = 640, F = 1728;
constexpr u32 O_XNT = 0;
constexpr u32 O_W1P = O_XNT + 2621440;
constexpr u32 O_W2P = O_W1P + 2211840;
constexpr u32 O_DP  = O_W2P + 2211840;
constexpr u32 O_PQ  = O_DP + 1638400;
constexpr u32 O_H   = O_PQ + 5242880;
constexpr u32 O_F   = O_H + 2621440;
constexpr u32 O_CS  = O_F + 7077888;
constexpr u32 O_ST  = O_CS + 27648;
constexpr u32 O_FL  = O_ST + 131072;
constexpr u32 O_NON = O_FL + 5 * 256 * 128;
constexpr u32 O_T0 = O_NON + 128; constexpr u32 WS_END = O_T0 + 1024;
constexpr int LDS_BYTES = 163840;
constexpr int SC1 = 16;
constexpr u32 SPIN_MAX = 1u << 18;
constexpr u32 L_CR = 0, L_XB = 65536, L_LN = 147456, L_WT = L_XB + 2 * 20480;
constexpr u32 L_RED = 102400, L_CS = 143360;
#define CI_EARLY 0
#define CR_EARLY 1
#define ROT (2 * j)
#define W2_N0 20
#define W2_N1 25

#define STAMP(i) do { if ((i) == 0) t00_ = __builtin_amdgcn_s_memrealtime(); if ((i) == HOLD_AT) { while (__builtin_amdgcn_s_memrealtime() - t00_ < HOLD_T) __builtin_amdgcn_s_sleep(1); } } while (0)

struct Params {
  const void *x, *cr, *ci, *dr, *di, *w1, *b1, *w2, *b2;
  void *out, *ws;
};

template <class Fn, int... I> DEV void sfor_(Fn&& f, std::integer_sequence<int, I...>) { (f(std::integral_constant<int, I>{}), ...); }
template <int N, class Fn> DEV void sfor(Fn&& f) { sfor_(f, std::make_integer_sequence<int, N>{}); }

DEV rsrc_t mkrs(const void* p, u32 bytes) { return __builtin_amdgcn_make_buffer_rsrc((void*)p, 0, (int)bytes, 0x00020000); }
template <int AUX> DEV u32x4 ld16(rsrc_t r, u32 v, u32 s) { return __builtin_amdgcn_raw_buffer_load_b128(r, v, s, AUX); }
template <int AUX> DEV u32x2 ld8(rsrc_t r, u32 v, u32 s) { return __builtin_amdgcn_raw_buffer_load_b64(r, v, s, AUX); }
template <int AUX> DEV void st16(u32x4 d, rsrc_t r, u32 v, u32 s) { __builtin_amdgcn_raw_buffer_store_b128(d, r, v, s, AUX); }
template <int AUX> DEV void st8(u32x2 d, rsrc_t r, u32 v, u32 s) { __builtin_amdgcn_raw_buffer_store_b64(d, r, v, s, AUX); }
template <int AUX> DEV void st4(u32 d, rsrc_t r, u32 v, u32 s) { __builtin_amdgcn_raw_buffer_store_b32(d, r, v, s, AUX); }
template <int IMM> DEV void setm0(u32 base) { asm volatile("s_add_u32 m0, %0, %1" ::"s"(base), "n"(IMM) : "scc"); }
template <int SC, int IMM> DEV void dmago(u32 voff, rsrc_t r, u32 soff) {
  if constexpr (SC) asm volatile("buffer_load_dwordx4 %0, %1, %2 offen offset:%3 sc1 lds" ::"v"(voff), "s"(r), "s"(soff), "n"(IMM) : "memory");
  else asm volatile("buffer_load_dwordx4 %0, %1, %2 offen offset:%3 lds" ::"v"(voff), "s"(r), "s"(soff), "n"(IMM) : "memory");
}
template <int SC, int MI> DEV void dma(u32 m0, u32 voff, rsrc_t r, u32 soff) {
  setm0<MI>(m0);
  asm volatile("s_nop 0");
  dmago<SC, 0>(voff, r, soff);
}
template <int R, int SC, int IMM> DEV void lda(u32 voff, rsrc_t r, u32 soff) {
  if constexpr (SC) asm volatile("buffer_load_dwordx4 a[%0:%1], %2, %3, %4 offen offset:%5 sc1" ::"n"(R), "n"(R + 3), "v"(voff), "s"(r), "s"(soff), "n"(IMM) : "memory");
  else asm volatile("buffer_load_dwordx4 a[%0:%1], %2, %3, %4 offen offset:%5" ::"n"(R), "n"(R + 3), "v"(voff), "s"(r), "s"(soff), "n"(IMM) : "memory");
}
template <int SC, int IMM> DEV void ldv(u32x4& d, u32 voff, rsrc_t r, u32 soff) {
  if constexpr (SC) asm volatile("buffer_load_dwordx4 %0, %1, %2, %3 offen offset:%4 sc1" : "=v"(d) : "v"(voff), "s"(r), "s"(soff), "n"(IMM) : "memory");
  else asm volatile("buffer_load_dwordx4 %0, %1, %2, %3 offen offset:%4" : "=v"(d) : "v"(voff), "s"(r), "s"(soff), "n"(IMM) : "memory");
}
template <int SC, int IMM> DEV void ldv8(u32x2& d, u32 voff, rsrc_t r, u32 soff) {
  if constexpr (SC) asm volatile("buffer_load_dwordx2 %0, %1, %2, %3 offen offset:%4 sc1" : "=v"(d) : "v"(voff), "s"(r), "s"(soff), "n"(IMM) : "memory");
  else asm volatile("buffer_load_dwordx2 %0, %1, %2, %3 offen offset:%4" : "=v"(d) : "v"(voff), "s"(r), "s"(soff), "n"(IMM) : "memory");
}
template <int N> DEV void waitv() { asm volatile("s_waitcnt vmcnt(%0)" ::"n"(N > 63 ? 63 : N) : "memory"); }
template <class T> DEV void landed(T& v) { asm volatile("; landed %0" : "+v"(v)); }
DEV void waitl0() { asm volatile("s_waitcnt lgkmcnt(0)" ::: "memory"); }
DEV void bar() { asm volatile("s_barrier" ::: "memory"); }
DEV void mf(f32x4& c, const u32x4& a, const u32x4& b) { asm volatile("v_mfma_f32_16x16x32_bf16 %0, %1, %2, %0" : "+v"(c) : "v"(a), "v"(b)); }
DEV void mf0(f32x4& c, const u32x4& a, const u32x4& b) { asm volatile("v_mfma_f32_16x16x32_bf16 %0, %1, %2, 0" : "=&v"(c) : "v"(a), "v"(b)); }
template <int R> DEV void mfB(f32x4& c, const u32x4& a) { asm volatile("v_mfma_f32_16x16x32_bf16 %0, %1, a[%2:%3], %0" : "+v"(c) : "v"(a), "n"(R), "n"(R + 3)); }
template <int R> DEV void mfB0(f32x4& c, const u32x4& a) { asm volatile("v_mfma_f32_16x16x32_bf16 %0, %1, a[%2:%3], 0" : "=&v"(c) : "v"(a), "n"(R), "n"(R + 3)); }
template <int R> DEV void mfA(f32x4& c, const u32x4& b) { asm volatile("v_mfma_f32_16x16x32_bf16 %0, a[%2:%3], %1, %0" : "+v"(c) : "v"(b), "n"(R), "n"(R + 3)); }
template <int R> DEV void mfA0(f32x4& c, const u32x4& b) { asm volatile("v_mfma_f32_16x16x32_bf16 %0, a[%2:%3], %1, 0" : "=&v"(c) : "v"(b), "n"(R), "n"(R + 3)); }
DEV void mfence5(f32x4 (&c)[5]) { asm volatile("s_nop 7" : "+v"(c[0]), "+v"(c[1]), "+v"(c[2]), "+v"(c[3]), "+v"(c[4])); }
DEV void mfence7(f32x4 (&c)[7]) { asm volatile("s_nop 7" : "+v"(c[0]), "+v"(c[1]), "+v"(c[2]), "+v"(c[3]), "+v"(c[4]), "+v"(c[5]), "+v"(c[6])); }

DEV u32x4 lds16(u32 a) { return *(const LAS u32x4*)(size_t)a; }
DEV u32x2 lds8(u32 a) { return *(const LAS u32x2*)(size_t)a; }
DEV void sts16(u32 a, u32x4 v) { *(LAS u32x4*)(size_t)a = v; }
DEV void sts4(u32 a, float v) { *(LAS float*)(size_t)a = v; }
DEV u32x4 tr16(u32 lo, u32 hi) {
  const u32x2 a = __builtin_bit_cast(u32x2, __builtin_amdgcn_ds_read_tr16_b64_v4i16((LAS s16x4*)(size_t)lo));
  const u32x2 b = __builtin_bit_cast(u32x2, __builtin_amdgcn_ds_read_tr16_b64_v4i16((LAS s16x4*)(size_t)hi));
  return u32x4{a[0], a[1], b[0], b[1]};
}
DEV u32 pk(float lo, float hi) { f32x2 v = {lo, hi}; return __builtin_bit_cast(u32, __builtin_convertvector(v, bf16x2)); }
DEV float blo(u32 u) { return __uint_as_float(u << 16); }
DEV float bhi(u32 u) { return __uint_as_float(u & 0xffff0000u); }
template <int CTRL> DEV float dppadd(float v) {
  return v + __uint_as_float(__builtin_amdgcn_update_dpp(0u, __float_as_uint(v), CTRL, 0xf, 0xf, true));
}
DEV float sum16(float v) { v = dppadd<0x128>(v); v = dppadd<0x124>(v); v = dppadd<0x122>(v); v = dppadd<0x121>(v); return v; }
DEV float addx16(float v) { u32 u = __float_as_uint(v); auto r = __builtin_amdgcn_permlane16_swap(u, u, false, false); return __uint_as_float(r[0]) + __uint_as_float(r[1]); }
DEV float addx32(float v) { u32 u = __float_as_uint(v); auto r = __builtin_amdgcn_permlane32_swap(u, u, false, false); return __uint_as_float(r[0]) + __uint_as_float(r[1]); }
DEV int blk5(int j, int i) { int v = i + (j & 1); return 5 * j + (v >= 5 ? v - 5 : v); }

DEV void publish(rsrc_t rws, int edge, int g, u32x2 tag, int lane) {
  if (lane == 0) st8<SC1>(tag, rws, 0, O_FL + (edge * 256 + g) * 128);
}
template <int NL> DEV void await(rsrc_t rws, u32 voff, u32 soff, u32x2 tag) {
  for (u32 spin = 0; spin < SPIN_MAX; ++spin) {
    u32x2 v[NL];
    sfor<NL>([&](auto C_) { ldv8<1, 0>(v[C_.value], voff, rws, soff + C_.value * 8192); });
    waitv<0>();
    bool ok = true;
#pragma unroll
    for (int c = 0; c < NL; ++c) { landed(v[c]); ok &= (v[c][0] == tag[0]) & (v[c][1] == tag[1]); }
    if (__builtin_amdgcn_ballot_w64(!ok) == 0) break;
  }
}

template <int NK, int PITCH, int DEPTH, int NB, class Mid>
DEV void ksplit(f32x4 (&acc)[5], const u32x4 (&wv)[10], rsrc_t rws, u32 sA, u32 vlast, u32 l0, int w, int lane, Mid&& mid) {
  u32x4 a[DEPTH][NK];
  const u32 v0 = lane * 16;
  auto loadA = [&](auto S_) {
    constexpr int s = S_.value;
    const u32 base = sA + ((w + 1 + s) & 3) * (PITCH * 1024);
    sfor<NK>([&](auto K_) { constexpr int k = K_.value; ldv<1, (k & 3) * 1024>(a[s % DEPTH][k], k == 13 ? vlast : v0, rws, base + (k >> 2) * 4096); });
  };
  sfor<DEPTH>(loadA);
  mid();
  sfor<4>([&](auto S_) {
    constexpr int s = S_.value;
    sfor<NK>([&](auto K_) {
      constexpr int k = K_.value;
      constexpr int issued = (s + DEPTH < 4 ? s + DEPTH : 4) * NK;
      waitv<issued - (s * NK + k + 1) + (s < DEPTH ? NB : 0)>();
      sfor<5>([&](auto I_) {
        constexpr int i = I_.value;
        if constexpr (k == 0) mfA0<4 * (5 * k + i)>(acc[i], a[s % DEPTH][k]);
        else if constexpr (k < 12) mfA<4 * (5 * k + i)>(acc[i], a[s % DEPTH][k]);
        else mf(acc[i], wv[5 * (k - 12) + i], a[s % DEPTH][k]);
      });
    });
    if constexpr (s + DEPTH < 4) loadA(std::integral_constant<int, s + DEPTH>{});
    mfence5(acc);
    if constexpr (s < 3) {
      const u32 dst = l0 + L_RED + (((w + 1 + s) & 3) * 3 + s) * 5120 + v0;
#pragma unroll
      for (int i = 0; i < 5; ++i) sts16(dst + i * 1024, __builtin_bit_cast(u32x4, acc[i]));
    }
    SB;
  });
  waitl0();
  bar();
  const u32 src = l0 + L_RED + w * 15360 + v0;
#pragma unroll
  for (int s = 0; s < 3; ++s)
#pragma unroll
    for (int i = 0; i < 5; ++i) acc[i] += __builtin_bit_cast(f32x4, lds16(src + s * 5120 + i * 1024));
}

__global__ __launch_bounds__(256, 1) void fnet_block(Params p_) {
  u64 t00_;
  const __attribute__((address_space(4))) Params* kp = (const __attribute__((address_space(4))) Params*)__builtin_amdgcn_kernarg_segment_ptr();
  asm volatile("s_memrealtime %0\n\ts_waitcnt lgkmcnt(0)" : "=s"(t00_), "+s"(kp)::"memory");
  Params p;
  p.x = kp->x; p.cr = kp->cr; p.ci = kp->ci; p.dr = kp->dr; p.di = kp->di; p.w1 = kp->w1; p.b1 = kp->b1; p.w2 = kp->w2; p.b2 = kp->b2; p.out = kp->out; p.ws = kp->ws;

  extern __shared__ __attribute__((aligned(16))) char smem[];
  asm volatile("" ::: "a0", "a63", "a127", "a191", "a255");
  const int b = blockIdx.x, w = __builtin_amdgcn_readfirstlane(threadIdx.x >> 6), lane = threadIdx.x & 63;
  const int l = b >> 3, sg = (b & 7) * 2 + (l >> 4), mi = l & 15, rg = sg * 2 + (mi >> 3), j = mi & 7, gid = sg * 16 + mi;
  const int t = lane & 15, q = lane >> 4;
  const u32 l0 = (u32)(size_t)(const LAS char*)smem, v0 = lane * 16;
  const rsrc_t rws = mkrs(p.ws, WS_END), rx = mkrs(p.x, S * D * 2), rcr = mkrs(p.cr, S * S * 2), rci = mkrs(p.ci, S * S * 2);

  u64 non;
  asm volatile("s_load_dwordx2 %0, %1, %2" : "=s"(non) : "s"(p.ws), "s"(O_NON) : "memory");
  const int rot = ROT;
  u32 va[4], vb[5];
#pragma unroll
  for (int i = 0; i < 4; ++i) { const u32 row = 4 * i + q; va[i] = (u32)(64 * rg + row) * 4096 + ((t ^ row) << 4); }
  const u32 vci = (u32)(64 * rg + 16 * w + t) * 4096 + q * 16;
  auto load_ci = [&](auto IT_) {
    constexpr int it = IT_.value;
    const u32 so = ((rot + it) & 15) * 256;
    sfor<4>([&](auto K_) { constexpr int k = K_.value; lda<16 * it + 4 * k, 0, 64 * k>(vci, rci, so); });
  };
  auto dma_cr = [&](auto IT_, int ww) {
    constexpr int it = IT_.value;
    const u32 so = ((rot + it) & 15) * 256 + ww * 65536, m = l0 + L_CR + ww * 4096;
    sfor<4>([&](auto N_) { constexpr int n = N_.value; dma<0, (it & 3) * 16384 + n * 1024>(m, va[n], rcr, so); });
  };

  const int hw = w - 2;
  const u32 lwt = l0 + L_WT + hw * 20480;
  if (w < 2) {
    u32x4 xv[5];
    const u32 v = (u32)(8 * b + 4 * w + q) * 1280 + t * 16;
#pragma unroll
    for (int c = 0; c < 5; ++c) xv[c] = ld16<0>(rx, v + c * 256, 0);
    float f[40], s = 0.f;
#pragma unroll
    for (int c = 0; c < 5; ++c)
#pragma unroll
      for (int e = 0; e < 4; ++e) { f[8 * c + 2 * e] = blo(xv[c][e]); f[8 * c + 2 * e + 1] = bhi(xv[c][e]); }
#pragma unroll
    for (int c = 0; c < 40; ++c) s += f[c];
    const float mean = sum16(s) * (1.f / D);
    float q2 = 0.f;
#pragma unroll
    for (int c = 0; c < 40; ++c) { f[c] -= mean; q2 += f[c] * f[c]; }
    const float rs = 1.f / sqrtf(sum16(q2) * (1.f / D) + 1e-5f);
    const u32 dst = l0 + L_LN + (4 * w + q) * 1280 + t * 16;
#pragma unroll
    for (int c = 0; c < 5; ++c)
      sts16(dst + c * 256, u32x4{pk(f[8 * c] * rs, f[8 * c + 1] * rs), pk(f[8 * c + 2] * rs, f[8 * c + 3] * rs), pk(f[8 * c + 4] * rs, f[8 * c + 5] * rs), pk(f[8 * c + 6] * rs, f[8 * c + 7] * rs)});
    waitl0();
  } else {
    if (b < 108) {
      const rsrc_t r = mkrs(p.w1, D * F * 2);
      const int cg = b % 27, kq = b / 27;
      const u32 v = (u32)(lane >> 2) * 3456 + (lane & 3) * 16, s0 = (u32)(160 * kq) * 3456 + cg * 128 + hw * 64;
      sfor<10>([&](auto I_) { constexpr int i = I_.value; dma<0, i * 1024>(lwt, v, r, s0 + i * 16 * 3456); });
    } else if (b < 216) {
      const rsrc_t r = mkrs(p.w2, D * F * 2);
      const int u = b - 108, ks = u >> 1, ch = u & 1;
      const u32 s0 = (u32)ks * 32 * 1280 + ch * 640 + hw * 320;
      sfor<10>([&](auto I_) {
        constexpr int i = I_.value;
        const u32 o = i * 64 + lane, row = o / 20;
        dma<0, i * 1024>(lwt, row * 1280 + (o - row * 20) * 16, r, s0);
      });
    } else {
      const rsrc_t r0 = mkrs(p.dr, D * D * 2), r1 = mkrs(p.di, D * D * 2);
      const u32 s0 = (u32)(b - 216) * 20480 + hw * 640;
      sfor<10>([&](auto I_) {
        constexpr int i = I_.value;
        const u32 o = i * 64 + lane, row = o / 40, c = o - row * 40;
        const u32 v = row * 1280 + ((c ^ ((row >> 1) & 7)) << 4);
        dma<0, i * 1024>(lwt, v, r0, s0);
        dma<0, 10240 + i * 1024>(lwt, v, r1, s0);
      });
    }
    if (CR_EARLY) sfor<4>([&](auto IT_) { dma_cr(IT_, hw); dma_cr(IT_, w); });
    sfor<CI_EARLY>(load_ci);
  }
  asm volatile("s_waitcnt lgkmcnt(0)" : "+s"(non)::"memory");
  non += 0x9E3779B97F4A7C15ull;
  const u32x2 tag = {(u32)non, (u32)(non >> 32)};
  STAMP(1);
  bar();
  STAMP(2);
  if (w == 0) {
    if (lane == 0) st4<SC1>((u32)t00_, rws, 0, O_T0 + b * 4);
    const u32 a = l0 + L_LN + (t >> 2) * 1280 + q * 32 + (t & 3) * 8;
#pragma unroll
    for (int c = 0; c < 10; ++c) st16<SC1>(tr16(a + c * 128, a + c * 128 + 4 * 1280), rws, v0 + (c & 3) * 1024, O_XNT + b * 10240 + (c >> 2) * 4096);
    waitv<0>();
    publish(rws, 0, b, tag, lane);
    STAMP(3);
  } else if (w >= 2) {
    waitv<(CR_EARLY ? 32 : 0) + 4 * CI_EARLY>();
    if (b < 108) {
      const int cg = b % 27, kq = b / 27;
      const u32 a = lwt + (4 * q + (t >> 2)) * 64 + (t & 3) * 8;
#pragma unroll
      for (int n = 0; n < 2; ++n) {
        float cs = 0.f;
#pragma unroll
        for (int i = 0; i < 5; ++i) {
          const u32x4 o = tr16(a + n * 32 + i * 2048, a + n * 32 + i * 2048 + 1024);
          st16<SC1>(o, rws, v0, O_W1P + ((cg * 4 + hw * 2 + n) * 20 + kq * 5 + i) * 1024);
#pragma unroll
          for (int e = 0; e < 4; ++e) cs += blo(o[e]) + bhi(o[e]);
        }
        cs = addx32(addx16(cs));
        if (lane < 16) st4<SC1>(__float_as_uint(cs), rws, v0, O_CS + (cg * 64 + hw * 32 + n * 16) * 16 + kq * 4);
      }
    } else if (b < 216) {
      const int u = b - 108, ks = u >> 1, ch = u & 1;
      const u32 a = lwt + (4 * q + (t >> 2)) * 320 + (t & 3) * 8;
#pragma unroll
      for (int i = 0; i < 10; ++i) st16<SC1>(tr16(a + i * 32, a + i * 32 + 16 * 320), rws, v0, O_W2P + ((ch * 20 + hw * 10 + i) * 54 + ks) * 1024);
    } else {
#pragma unroll
      for (int i = 0; i < 20; ++i) {
        const u32 c = 2 * i + (q >> 1);
        const u32 a = lwt + t * 640 + ((c ^ (t >> 1)) << 4) + (q & 1) * 8;
        const u32x2 re = lds8(a), im = lds8(a + 10240);
        st16<SC1>(u32x4{re[0], re[1], im[0] ^ 0x80008000u, im[1] ^ 0x80008000u}, rws, v0 + (i & 3) * 1024, O_DP + ((b - 216) * 40 + hw * 20 + (i & ~3)) * 1024);
      }
    }
    waitl0();
  }
  if (!CR_EARLY) sfor<4>([&](auto IT_) { dma_cr(IT_, w); });
  if (CI_EARLY > 0 && w < 2) sfor<CI_EARLY>(load_ci);
  sfor<16 - CI_EARLY>([&](auto I_) { load_ci(std::integral_constant<int, I_.value + CI_EARLY>{}); });
  STAMP(4);
  if (w == 3) await<4>(rws, lane * 128, O_FL, tag);
  STAMP(5);
  bar();
  STAMP(6);

#pragma unroll
  for (int i = 0; i < 5; ++i) { const u32 o = (u32)(w * 5 + i) * 64 + lane, sb = o / 80; vb[i] = sb * 10240 + (o - sb * 80) * 16; }
  const u32 mB = l0 + L_XB + w * 5120, mA = l0 + L_CR + w * 4096, sB = O_XNT + j * 1280;
  sfor<3>([&](auto IT_) {
    constexpr int it = IT_.value;
    const u32 so = sB + ((rot + it) & 15) * 163840;
    sfor<5>([&](auto N_) { constexpr int n = N_.value; dma<1, it * 20480 + n * 1024>(mB, vb[n], rws, so); });
  });
  f32x4 aP[5], aQ[5];
  u32x4 fr[2][6];
  u32x2 wfl;
  u32 ra[4];
#pragma unroll
  for (int k = 0; k < 4; ++k) ra[k] = l0 + L_CR + w * 4096 + t * 256 + (((4 * k + q) ^ t) << 4);
  const u32 rb0 = l0 + L_XB + q * 1280 + t * 16, rb1 = rb0 + 40960;
  u32 sD[5];
#pragma unroll
  for (int i = 0; i < 5; ++i) sD[i] = O_DP + (blk5(j, i) * 40 + 10 * w) * 1024;
  auto load_d = [&](auto N_) {
    constexpr int n = N_.value, k = n / 5, i = n % 5;
    lda<4 * n, 1, (k & 3) * 1024>(v0, rws, sD[i] + (k >> 2) * 4096);
  };
  auto cnt = [](int it) { return it < 0 ? 5 : it == 0 ? 5 : it == 8 ? 10 : 9; };
  sfor<16>([&](auto IT_) {
    constexpr int it = IT_.value;
    waitv<cnt(it - 2) + cnt(it - 1)>();
    waitl0();
    bar();
    if constexpr (it == 0) {
      STAMP(7);
      if (w == 0) { publish(rws, 1, b, tag, lane); if (b == 0 && lane == 0) st8<SC1>(tag, rws, 0, O_NON); }
    }
    if constexpr (it == 11) {
      landed(wfl);
      if (__builtin_amdgcn_ballot_w64(wfl[0] != tag[0] || wfl[1] != tag[1]) != 0) await<1>(rws, lane * 128, O_FL + (256 + 64 * w) * 128, tag);
    }
    const u32 kn = (rot + it + 3) & 15, soA = kn * 256 + w * 65536, soB = sB + kn * 163840;
    SB;
    sfor<4>([&](auto K_) {
      constexpr int K = K_.value, U = (K + 1) & 1, G = 4 * it + K - 1;
      sfor<10>([&](auto G_) {
        constexpr int g = G_.value, i = g >> 1, m = 10 * K + g;
        if constexpr (G == 0) { if constexpr (g & 1) mfB0<4 * G>(aQ[i], fr[U][1 + i]); else mf0(aP[i], fr[U][1 + i], fr[U][0]); }
        else if constexpr (G > 0) { if constexpr (g & 1) mfB<4 * G>(aQ[i], fr[U][1 + i]); else mf(aP[i], fr[U][1 + i], fr[U][0]); }
        SB;
        if constexpr (g < 6) {
          constexpr int r = g == 0 ? 1 : g == 1 ? 0 : g;
          if constexpr (r == 0) fr[U ^ 1][0] = lds16(ra[K] + (it & 3) * 16384);
          else fr[U ^ 1][r] = lds16(((it & 2) ? rb1 : rb0) + (it & 1) * 20480 + K * 5120 + (r - 1) * 256);
        }
        constexpr int n = m / 4;
        if constexpr (it < 13 && n < (it == 0 ? 5 : 9)) {
          if constexpr (m % 4 == 2) { if constexpr (n < 5) setm0<((it + 3) & 3) * 20480 + n * 1024>(mB); else setm0<((it + 3) & 3) * 16384 + (n - 5) * 1024>(mA); }
          if constexpr (m % 4 == 3) { if constexpr (n < 5) dmago<1, 0>(vb[n], rws, soB); else dmago<0, 0>(va[n - 5], rcr, soA); }
        }
        if constexpr (it == 8 && m == 39) ldv8<1, 0>(wfl, lane * 128, rws, O_FL + (256 + 64 * w) * 128);
        if constexpr (it >= 13 && n < 9 && m % 4 == 3) load_d(std::integral_constant<int, 9 * (it - 13) + n>{});
        SB;
      });
    });
  });
  sfor<5>([&](auto I_) { constexpr int i = I_.value; mf(aP[i], fr[1][1 + i], fr[1][0]); mfB<252>(aQ[i], fr[1][1 + i]); });
  mfence5(aP);
  mfence5(aQ);
  STAMP(8);
#pragma unroll
  for (int i = 0; i < 5; ++i) {
    const u32x4 o = {pk(aP[i][0], aP[i][1]), pk(aP[i][2], aP[i][3]), pk(aQ[i][0], aQ[i][1]), pk(aQ[i][2], aQ[i][3])};
    st16<SC1>(o, rws, v0 + (i & 3) * 1024, O_PQ + ((4 * rg + w) * 40 + 5 * j + (i & ~3)) * 1024);
  }
  SB;
  sfor<23>([&](auto N_) { load_d(std::integral_constant<int, 27 + N_.value>{}); });
  waitv<23>();
  STAMP(9);
  bar();
  if (w == 0) publish(rws, 2, gid, tag, lane);
  await<1>(rws, (lane & 1) * 128, O_FL + (512 + rg * 8 + 2 * w) * 128, tag);
  STAMP(10);

  const int nb0 = mi < 12 ? 7 * mi : 84 + 6 * (mi - 12), odd = mi < 12 ? (mi & 1) : 0;
  auto nbi = [&](int i) { int v = i + odd; v = v >= 7 ? 0 : v; return mi >= 12 && i == 6 ? nb0 : nb0 + v; };
  auto dma_w1 = [&](auto I_) {
    constexpr int i = I_.value;
    const u32 m = l0 + 5120 * w, s = O_W1P + (nbi(i) * 20 + 5 * w) * 1024;
    setm0<i * 20480>(m);
    asm volatile("s_nop 0");
    dmago<1, 0>(v0, rws, s); dmago<1, 1024>(v0, rws, s); dmago<1, 2048>(v0, rws, s); dmago<1, 3072>(v0, rws, s);
    dma<1, i * 20480 + 4096>(m, v0, rws, s + 4096);
  };
  const int k0 = w < 2 ? 14 * w : 28 + 13 * (w - 2);
  const u32 vlast = w < 2 ? v0 : 0x7f000000u;
  u32 sW[5];
#pragma unroll
  for (int i = 0; i < 5; ++i) sW[i] = O_W2P + (blk5(j, i) * 54 + k0) * 1024;
  auto load_w2 = [&](auto N_) {
    constexpr int n = N_.value, k = n / 5, i = n % 5;
    lda<4 * n, 1, (k & 3) * 1024>(v0, rws, sW[i] + (k >> 2) * 4096);
  };
  u32x4 wv[10];
  const u32 hb = O_H + (4 * rg + w) * 20480;
  const int kp5 = (5 * j + 1) >> 1;
  {
    u32x2 xr[5];
    f32x4 acc[5];
    ksplit<10, 40, 4, 30>(acc, wv, rws, O_PQ + (4 * rg * 40 + 10 * w) * 1024, v0, l0, w, lane, [&]() {
      sfor<5>([&](auto I_) { ldv8<0, 0>(xr[I_.value], (u32)t * 1280 + q * 8, rx, (u32)(64 * rg + 16 * w) * 1280 + blk5(j, I_.value) * 32); });
      sfor<5>(dma_w1);
    });
    STAMP(11);
    waitv<25>();
    u32x2 h[5];
    float s1 = 0.f, s2 = 0.f;
#pragma unroll
    for (int i = 0; i < 5; ++i) {
      landed(xr[i]);
      h[i][0] = pk(acc[i][0] + blo(xr[i][0]), acc[i][1] + bhi(xr[i][0]));
      h[i][1] = pk(acc[i][2] + blo(xr[i][1]), acc[i][3] + bhi(xr[i][1]));
    }
    st16<SC1>(u32x4{h[0][0], h[0][1], h[1][0], h[1][1]}, rws, v0, hb + kp5 * 1024);
    st16<SC1>(u32x4{h[2][0], h[2][1], h[3][0], h[3][1]}, rws, v0 + 1024, hb + kp5 * 1024);
    st8<SC1>(h[4], rws, v0, hb + (blk5(j, 4) >> 1) * 1024 + (j & 1) * 8);
#pragma unroll
    for (int i = 0; i < 5; ++i) {
      const float a0 = blo(h[i][0]), a1 = bhi(h[i][0]), a2 = blo(h[i][1]), a3 = bhi(h[i][1]);
      s1 += (a0 + a1) + (a2 + a3);
      s2 += a0 * a0 + a1 * a1 + a2 * a2 + a3 * a3;
    }
    s1 = addx32(addx16(s1));
    s2 = addx32(addx16(s2));
    if (lane < 16) st8<SC1>(u32x2{__float_as_uint(s1), __float_as_uint(s2)}, rws, lane * 64, O_ST + (64 * rg + 16 * w) * 64 + j * 8);
  }
  SB;
  sfor<W2_N0>(load_w2);
  waitv<W2_N0>();
  waitl0();
  STAMP(12);
  bar();
  if (w == 0) publish(rws, 3, gid, tag, lane);
  dma_w1(std::integral_constant<int, 5>{});
  dma_w1(std::integral_constant<int, 6>{});
  {
    const int tid = w * 64 + lane;
    if (tid < 112) {
      const rsrc_t rb1s = mkrs(p.b1, F * 2);
      const int i = tid >> 4;
      int v = i + odd; v = v >= 7 ? 0 : v; v = (mi >= 12 && i == 6) ? 0 : v;
      const u32 col = (u32)(nb0 + v) * 16 + (tid & 15);
      const u32x4 c = ld16<SC1>(rws, col * 16, O_CS);
      const u32 bb = __builtin_amdgcn_raw_buffer_load_b16(rb1s, col * 2, 0, 0);
      sts4(l0 + L_CS + tid * 4, (__uint_as_float(c[0]) + __uint_as_float(c[1])) + (__uint_as_float(c[2]) + __uint_as_float(c[3])));
      sts4(l0 + L_CS + 448 + tid * 4, blo(bb));
    }
  }
  await<1>(rws, (lane & 7) * 128, O_FL + (768 + sg * 16 + (w >> 1) * 8) * 128, tag);
  STAMP(13);

  {
    u32x4 a[20][2], stv[2];
    sfor<2>([&](auto M_) { ldv<1, 0>(stv[M_.value], t * 64 + q * 16, rws, O_ST + (128 * sg + 32 * w + 16 * M_.value) * 64); });
    sfor<20>([&](auto K_) {
      constexpr int k = K_.value;
      sfor<2>([&](auto M_) { ldv<1, (k & 3) * 1024>(a[k][M_.value], v0, rws, O_H + ((8 * sg + 2 * w + M_.value) * 20 + (k & ~3)) * 1024); });
    });
    waitl0();
    bar();
    STAMP(14);
    f32x4 acc[2][7];
    u32x4 wq[4];
    const u32 lb0 = l0 + v0, lb1 = lb0 + 61440, lb2 = lb0 + 122880;
    auto rdw = [&](int n) { const int k = n / 7, i = n % 7, o = (i * 20 + k) * 1024; return o < 61440 ? lds16(lb0 + o) : o < 122880 ? lds16(lb1 + (o - 61440)) : lds16(lb2 + (o - 122880)); };
#pragma unroll
    for (int n = 0; n < 3; ++n) wq[n] = rdw(n);
    SB;
    sfor<140>([&](auto N_) {
      constexpr int n = N_.value, k = n / 7, i = n % 7;
      constexpr int nw = (n + 3) / 5 < W2_N1 ? (n + 3) / 5 : W2_N1;
      if constexpr (i == 0) waitv<38 - 2 * k + nw>();
      if constexpr (k == 0) mf0(acc[0][i], wq[n & 3], a[k][0]); else mf(acc[0][i], wq[n & 3], a[k][0]);
      SB;
      if constexpr (n + 3 < 140) wq[(n + 3) & 3] = rdw(n + 3);
      SB;
      if constexpr (k == 0) mf0(acc[1][i], wq[n & 3], a[k][1]); else mf(acc[1][i], wq[n & 3], a[k][1]);
      if constexpr (n % 5 == 1 && n / 5 < W2_N1) load_w2(std::integral_constant<int, W2_N0 + n / 5>{});
      SB;
    });
    mfence7(acc[0]);
    mfence7(acc[1]);
    STAMP(15);
    float mean[2], rstd[2];
#pragma unroll
    for (int m = 0; m < 2; ++m) {
      landed(stv[m]);
      const float s1 = addx32(addx16(__uint_as_float(stv[m][0]) + __uint_as_float(stv[m][2])));
      const float s2 = addx32(addx16(__uint_as_float(stv[m][1]) + __uint_as_float(stv[m][3])));
      mean[m] = s1 * (1.f / D);
      const float var = fmaxf(s2 * (1.f / D) - mean[m] * mean[m], 0.f);
      rstd[m] = 1.f / sqrtf(var + 1e-5f);
    }
    u32x2 fo[2][7];
#pragma unroll
    for (int i = 0; i < 7; ++i) {
      const f32x4 cs = __builtin_bit_cast(f32x4, lds16(l0 + L_CS + i * 64 + q * 16)), bb = __builtin_bit_cast(f32x4, lds16(l0 + L_CS + 448 + i * 64 + q * 16));
#pragma unroll
      for (int m = 0; m < 2; ++m) {
        float v[4];
#pragma unroll
        for (int e = 0; e < 4; ++e) { const float z = (acc[m][i][e] - mean[m] * cs[e]) * rstd[m] + bb[e]; v[e] = fmaxf(z, 0.f) * z; }
        fo[m][i] = u32x2{pk(v[0], v[1]), pk(v[2], v[3])};
      }
    }
    const int kp = (nb0 + 1) >> 1;
#pragma unroll
    for (int m = 0; m < 2; ++m) {
      const u32 fb = O_F + (8 * sg + 2 * w + m) * 55296;
#pragma unroll
      for (int c = 0; c < 3; ++c) st16<SC1>(u32x4{fo[m][2 * c][0], fo[m][2 * c][1], fo[m][2 * c + 1][0], fo[m][2 * c + 1][1]}, rws, v0 + c * 1024, fb + kp * 1024);
      if (mi < 12) st8<SC1>(fo[m][6], rws, v0, fb + (nbi(6) >> 1) * 1024 + odd * 8);
    }
  }
  SB;
  constexpr int W2_N2 = 60 - W2_N0 - W2_N1;
  sfor<W2_N2>([&](auto N_) { load_w2(std::integral_constant<int, W2_N0 + W2_N1 + N_.value>{}); });
  sfor<10>([&](auto N_) { constexpr int n = N_.value; ldv<1, (n / 5) * 1024>(wv[n], n < 5 ? v0 : vlast, rws, sW[n % 5] + 3 * 4096); });
  u32x4 h01, h23;
  u32x2 h4, bv[5];
  ldv<1, 0>(h01, v0, rws, hb + kp5 * 1024);
  ldv<1, 1024>(h23, v0, rws, hb + kp5 * 1024);
  ldv8<1, 0>(h4, v0, rws, hb + (blk5(j, 4) >> 1) * 1024 + (j & 1) * 8);
  {
    const rsrc_t rb2 = mkrs(p.b2, D * 2);
    sfor<5>([&](auto I_) { ldv8<0, 0>(bv[I_.value], q * 8, rb2, blk5(j, I_.value) * 32); });
  }
  waitv<W2_N2 + 18>();
  waitl0();
  STAMP(16);
  bar();
  if (w == 0) publish(rws, 4, gid, tag, lane);
  await<1>(rws, t * 128, O_FL + (1024 + sg * 16) * 128, tag);
  STAMP(17);

  {
    f32x4 acc[5];
    ksplit<14, 54, 2, 0>(acc, wv, rws, O_F + (4 * rg * 54 + k0) * 1024, vlast, l0, w, lane, []() {});
    STAMP(18);
    landed(h01); landed(h23); landed(h4);
    const rsrc_t rout = mkrs(p.out, S * D * 2);
    const u32x2 h[5] = {{h01[0], h01[1]}, {h01[2], h01[3]}, {h23[0], h23[1]}, {h23[2], h23[3]}, h4};
#pragma unroll
    for (int i = 0; i < 5; ++i) {
      landed(bv[i]);
      u32x2 o;
      o[0] = pk(acc[i][0] + blo(bv[i][0]) + blo(h[i][0]), acc[i][1] + bhi(bv[i][0]) + bhi(h[i][0]));
      o[1] = pk(acc[i][2] + blo(bv[i][1]) + blo(h[i][1]), acc[i][3] + bhi(bv[i][1]) + bhi(h[i][1]));
      st8<0>(o, rout, (u32)t * 1280 + q * 8, (u32)(64 * rg + 16 * w) * 1280 + blk5(j, i) * 32);
    }
  }
  {
    const u32x4 tv = ld16<SC1>(rws, v0, O_T0);
    int d = max((int)(tv[0] - (u32)t00_), max((int)(tv[1] - (u32)t00_), max((int)(tv[2] - (u32)t00_), (int)(tv[3] - (u32)t00_))));
    for (int o = 1; o < 64; o <<= 1) d = max(d, __shfl_xor(d, o));
    t00_ += (long long)__builtin_amdgcn_readfirstlane(d);
  }
  STAMP(19);
}

extern "C" void kernel_launch(void* const* d_in, const int* in_sizes, int n_in, void* d_out, int out_size, void* d_ws, size_t ws_size,
                              hipStream_t stream) {
  Params p;
  p.x = d_in[0]; p.cr = d_in[1]; p.ci = d_in[2]; p.dr = d_in[3]; p.di = d_in[4];
  p.w1 = d_in[6]; p.b1 = d_in[7]; p.w2 = d_in[8]; p.b2 = d_in[9];
  p.out = d_out; p.ws = d_ws;
  (void)hipFuncSetAttribute((const void*)fnet_block, hipFuncAttributeMaxDynamicSharedMemorySize, LDS_BYTES);
  fnet_block<<<256, 256, LDS_BYTES, stream>>>(p);
}
```

```cpp
#include <hip/hip_runtime.h>
#include <hip/hip_bf16.h>
#include <type_traits>
#include <utility>

typedef unsigned short u16;
typedef unsigned int u32;
typedef unsigned long long u64;
typedef __attribute__((ext_vector_type(4))) u32 u32x4;
typedef __attribute__((ext_vector_type(2))) u32 u32x2;
typedef __attribute__((ext_vector_type(4))) float f32x4;
typedef __attribute__((ext_vector_type(2))) float f32x2;
typedef __attribute__((ext_vector_type(2))) __bf16 bf16x2;
typedef __attribute__((ext_vector_type(4))) short s16x4;
typedef __amdgpu_buffer_rsrc_t rsrc_t;
#define LAS __attribute__((address_space(3)))
#define DEV static __device__ __forceinline__
#define SB __builtin_amdgcn_sched_barrier(0)

constexpr int S = 2048, D = 640, F = 1728;
#define FS0 8
#define FS1 128
constexpr u32 O_XNT = 0;
constexpr u32 O_W1P = O_XNT + 2621440;
constexpr u32 O_W2P = O_W1P + 2211840;
constexpr u32 O_DP  = O_W2P + 2211840;
constexpr u32 O_PQ  = O_DP + 1638400;
constexpr u32 O_H   = O_PQ + 5242880;
constexpr u32 O_F   = O_H + 2621440;
constexpr u32 O_CS  = O_F + 7077888;
constexpr u32 O_ST  = O_CS + 55296;
constexpr u32 O_FL  = O_ST + 131072;
constexpr u32 O_FW  = O_FL + 2 * 256 * FS0;
constexpr u32 O_NON = O_FW + 3 * 256 * FS1;
constexpr u32 WS_END = O_NON + 128;
constexpr int LDS_BYTES = 163840;
constexpr int SC1 = 16;
constexpr u32 SPIN_MAX = 1u << 18;
constexpr u32 L_CR = 0, L_XB = 65536, L_LN = 147456, L_WT = L_XB + 2 * 20480;
constexpr u32 L_RED = 122880, L_CS = 143360;
#define LA 2
#define UA 2
#define UB 0
#define U0 (UA + UB)
#define U1 3
#define NE 4
#define NX 2
#define RT 11
#define UP 8
#define UX 10
#define ROT (2 * j)
#define TA 0
#define TI 0
#define NTA 0
#define TB 1
#define NTW 0
#define NTI 0
#define NTR 0
#define NTXX 0
#define TW 1
#define NB 4
#define TO 0
#define TW2 0
#define XD 0
#define XS 14
#define SX 0
#define SB2 15
#define SCS 13
#define BL 0
#define HB2 3
#define HB3 2
#define HB4 2
#define EPA 0
#define EP0 6
#define EP1 8
#define EP2 0
#define PORD 0
#define NPC (14 + (TA > 2))
#define RING 12
#define X21 12
#define X22 18
#define X23 5
#define X31 12
#define X32 14
#define X33 0
#define X41 8
#define X42 18
#define R3 5
#define REMAP 1
#define REMAP1 1
#define ROT4 ((rg1 & 1) * 6)
#define FL1 11
#define STG 2
#define WSP 1
#define K1 10
#define KX 6
#define NPF3 34
#define PRO 11
#define TAILR 1
#define CEND 1
#define CPF 2
#define OUT_AUX 0

struct Params {
  const void *x, *cr, *ci, *dr, *di, *w1, *b1, *w2, *b2;
  void *out, *ws;
};

template <class Fn, int... I> DEV void sfor_(Fn&& f, std::integer_sequence<int, I...>) { (f(std::integral_constant<int, I>{}), ...); }
template <int N, class Fn> DEV void sfor(Fn&& f) { sfor_(f, std::make_integer_sequence<int, N>{}); }

DEV rsrc_t mkrs(const void* p, u32 bytes) { return __builtin_amdgcn_make_buffer_rsrc((void*)p, 0, (int)bytes, 0x00020000); }
template <int AUX> DEV u32x4 ld16(rsrc_t r, u32 v, u32 s) { return __builtin_amdgcn_raw_buffer_load_b128(r, v, s, AUX); }
template <int AUX> DEV u32x2 ld8(rsrc_t r, u32 v, u32 s) { return __builtin_amdgcn_raw_buffer_load_b64(r, v, s, AUX); }
template <int AUX> DEV void st16(u32x4 d, rsrc_t r, u32 v, u32 s) { __builtin_amdgcn_raw_buffer_store_b128(d, r, v, s, AUX); }
template <int AUX> DEV void st8(u32x2 d, rsrc_t r, u32 v, u32 s) { __builtin_amdgcn_raw_buffer_store_b64(d, r, v, s, AUX); }
template <int AUX> DEV void st4(u32 d, rsrc_t r, u32 v, u32 s) { __builtin_amdgcn_raw_buffer_store_b32(d, r, v, s, AUX); }
template <int IMM> DEV void setm0(u32 base) { asm volatile("s_add_u32 m0, %0, %1" ::"s"(base), "n"(IMM) : "scc"); }
template <int SC, int IMM> DEV void dmago(u32 voff, rsrc_t r, u32 soff) {
  if constexpr (SC == 2) asm volatile("buffer_load_dwordx4 %0, %1, %2 offen offset:%3 nt lds" ::"v"(voff), "s"(r), "s"(soff), "n"(IMM) : "memory");
  else if constexpr (SC) asm volatile("buffer_load_dwordx4 %0, %1, %2 offen offset:%3 sc1 lds" ::"v"(voff), "s"(r), "s"(soff), "n"(IMM) : "memory");
  else asm volatile("buffer_load_dwordx4 %0, %1, %2 offen offset:%3 lds" ::"v"(voff), "s"(r), "s"(soff), "n"(IMM) : "memory");
}
template <int SC, int MI> DEV void dma(u32 m0, u32 voff, rsrc_t r, u32 soff) {
  setm0<MI>(m0);
  asm volatile("s_nop 0");
  dmago<SC, 0>(voff, r, soff);
}
template <int R, int SC, int IMM> DEV void lda(u32 voff, rsrc_t r, u32 soff) {
  if constexpr (SC) asm volatile("buffer_load_dwordx4 a[%0:%1], %2, %3, %4 offen offset:%5 sc1" ::"n"(R), "n"(R + 3), "v"(voff), "s"(r), "s"(soff), "n"(IMM) : "memory");
  else asm volatile("buffer_load_dwordx4 a[%0:%1], %2, %3, %4 offen offset:%5" ::"n"(R), "n"(R + 3), "v"(voff), "s"(r), "s"(soff), "n"(IMM) : "memory");
}
template <int SC, int IMM> DEV void ldv(u32x4& d, u32 voff, rsrc_t r, u32 soff) {
  if constexpr (SC) asm volatile("buffer_load_dwordx4 %0, %1, %2, %3 offen offset:%4 sc1" : "=v"(d) : "v"(voff), "s"(r), "s"(soff), "n"(IMM) : "memory");
  else asm volatile("buffer_load_dwordx4 %0, %1, %2, %3 offen offset:%4" : "=v"(d) : "v"(voff), "s"(r), "s"(soff), "n"(IMM) : "memory");
}
template <int SC, int IMM> DEV void ldv8(u32x2& d, u32 voff, rsrc_t r, u32 soff) {
  if constexpr (SC == 2) asm volatile("buffer_load_dwordx2 %0, %1, %2, %3 offen offset:%4 nt" : "=v"(d) : "v"(voff), "s"(r), "s"(soff), "n"(IMM) : "memory");
  else if constexpr (SC) asm volatile("buffer_load_dwordx2 %0, %1, %2, %3 offen offset:%4 sc1" : "=v"(d) : "v"(voff), "s"(r), "s"(soff), "n"(IMM) : "memory");
  else asm volatile("buffer_load_dwordx2 %0, %1, %2, %3 offen offset:%4" : "=v"(d) : "v"(voff), "s"(r), "s"(soff), "n"(IMM) : "memory");
}
DEV void ldu16(u32& d, u32 voff, rsrc_t r) { asm volatile("buffer_load_ushort %0, %1, %2, 0 offen" : "=v"(d) : "v"(voff), "s"(r) : "memory"); }
template <int IMM> DEV void sta16(const u32x4& d, u32 voff, rsrc_t r, u32 soff) { asm volatile("buffer_store_dwordx4 %0, %1, %2, %3 offen offset:%4 sc1" ::"v"(d), "v"(voff), "s"(r), "s"(soff), "n"(IMM) : "memory"); }
DEV void sta8(const u32x2& d, u32 voff, rsrc_t r, u32 soff) { asm volatile("buffer_store_dwordx2 %0, %1, %2, %3 offen sc1" ::"v"(d), "v"(voff), "s"(r), "s"(soff) : "memory"); }
DEV void sta4(u32 d, u32 voff, rsrc_t r, u32 soff) { asm volatile("buffer_store_dword %0, %1, %2, %3 offen sc1" ::"v"(d), "v"(voff), "s"(r), "s"(soff) : "memory"); }
DEV void touch(u32& d, u32 voff, rsrc_t r, u32 soff) { asm volatile("buffer_load_dword %0, %1, %2, %3 offen sc1" : "+v"(d) : "v"(voff), "s"(r), "s"(soff) : "memory"); }
DEV void touch0(u32& d, u32 voff, rsrc_t r, u32 soff) { asm volatile("buffer_load_dword %0, %1, %2, %3 offen" : "+v"(d) : "v"(voff), "s"(r), "s"(soff) : "memory"); }
template <int N> DEV void waitv() { asm volatile("s_waitcnt vmcnt(%0)" ::"n"(N > 63 ? 63 : N) : "memory"); }
template <class T> DEV void landed(T& v) { asm volatile("; landed %0" : "+v"(v)); }
template <int N> DEV void waitl() { asm volatile("s_waitcnt lgkmcnt(%0)" ::"n"(N) : "memory"); }
template <int IMM> DEV void dsr(u32x4& d, u32 a) { asm volatile("ds_read_b128 %0, %1 offset:%2" : "=v"(d) : "v"(a), "n"(IMM) : "memory"); }
template <int IMM> DEV void dsrf(f32x4& d, u32 a) { asm volatile("ds_read_b128 %0, %1 offset:%2" : "=v"(d) : "v"(a), "n"(IMM) : "memory"); }
template <int IMM> DEV void dsw(u32 a, const f32x4& d) { asm volatile("ds_write_b128 %0, %1 offset:%2" ::"v"(a), "v"(d), "n"(IMM) : "memory"); }
template <int HI> DEV float upk(u32 v) {
  float d;
  if constexpr (HI) asm volatile("v_and_b32 %0, 0xffff0000, %1" : "=v"(d) : "v"(v)); else asm volatile("v_lshlrev_b32 %0, 16, %1" : "=v"(d) : "v"(v));
  return d;
}
DEV void vadd(float& d, float a) { asm volatile("v_add_f32 %0, %0, %1" : "+v"(d) : "v"(a)); }
DEV void vsqrt(float& d) { asm volatile("v_sqrt_f32 %0, %0" : "+v"(d)); }
template <int NEG> DEV void vkeep16(float& d) {
  if constexpr (NEG) asm volatile("v_cndmask_b32 %0, 0, -%0, %1" : "+v"(d) : "s"(0xffffull)); else asm volatile("v_cndmask_b32 %0, 0, %0, %1" : "+v"(d) : "s"(0xffffull));
}
DEV void vcvt(u32& d, float a, float b) { asm volatile("v_cvt_pk_bf16_f32 %0, %1, %2" : "=v"(d) : "v"(a), "v"(b)); }
DEV void vcvt0(u32& d, float a) { asm volatile("v_cvt_pk_bf16_f32 %0, %1, 0" : "=v"(d) : "v"(a)); }
DEV void vrsub(float& d, float a) { asm volatile("v_sub_f32 %0, %1, %0" : "+v"(d) : "v"(a)); }
DEV void vsqa(float& d, float a) { asm volatile("v_mul_f32 %0, %1, |%1|" : "=v"(d) : "v"(a)); }
DEV void vrelu2(u32& d) { asm volatile("v_pk_max_i16 %0, %0, 0" : "+v"(d)); }
template <u32 C> DEV void vaddi(u32& d, u32 a) { asm volatile("v_add_u32 %0, %1, %2" : "=v"(d) : "n"(C), "v"(a)); }
DEV void vminu(u32& d, u32 a) { asm volatile("v_min_u32 %0, %0, %1" : "+v"(d) : "v"(a)); }
DEV void vrcp(float& d) { asm volatile("v_rcp_f32 %0, %0" : "+v"(d)); }
DEV void vfma(float& d, float a, float b) { asm volatile("v_fmac_f32 %0, %1, %2" : "+v"(d) : "v"(a), "v"(b)); }
template <int R, int IMM> DEV void dsra(u32 a) { asm volatile("ds_read_b128 a[%0:%1], %2 offset:%3" ::"n"(R), "n"(R + 3), "v"(a), "n"(IMM) : "memory"); }
template <int R> DEV void toa(u32 v) { asm volatile("v_accvgpr_write_b32 a[%0], %1" ::"n"(R), "v"(v)); }
template <int R> DEV u32 froma() { u32 v; asm volatile("v_accvgpr_read_b32 %0, a[%1]" : "=v"(v) : "n"(R)); return v; }
DEV void snext(u32& s) { asm volatile("s_add_u32 %0, %0, 0x100\n\ts_and_b32 %0, %0, 0xf00" : "+s"(s)::"scc"); }
DEV void brk() { asm volatile("s_nop 0"); }
DEV void waitl0() { asm volatile("s_waitcnt lgkmcnt(0)" ::: "memory"); }
DEV void bar() { asm volatile("s_barrier" ::: "memory"); }
DEV void mf(f32x4& c, const u32x4& a, const u32x4& b) { asm volatile("v_mfma_f32_16x16x32_bf16 %0, %1, %2, %0" : "+v"(c) : "v"(a), "v"(b)); }
DEV void mf0(f32x4& c, const u32x4& a, const u32x4& b) { asm volatile("v_mfma_f32_16x16x32_bf16 %0, %1, %2, 0" : "=&v"(c) : "v"(a), "v"(b)); }
template <int R> DEV void mfB(f32x4& c, const u32x4& a) { asm volatile("v_mfma_f32_16x16x32_bf16 %0, %1, a[%2:%3], %0" : "+v"(c) : "v"(a), "n"(R), "n"(R + 3)); }
template <int R> DEV void mfB0(f32x4& c, const u32x4& a) { asm volatile("v_mfma_f32_16x16x32_bf16 %0, %1, a[%2:%3], 0" : "=&v"(c) : "v"(a), "n"(R), "n"(R + 3)); }
template <int R> DEV void mfA(f32x4& c, const u32x4& b) { asm volatile("v_mfma_f32_16x16x32_bf16 %0, a[%2:%3], %1, %0" : "+v"(c) : "v"(b), "n"(R), "n"(R + 3)); }
template <int R> DEV void mfA0(f32x4& c, const u32x4& b) { asm volatile("v_mfma_f32_16x16x32_bf16 %0, a[%2:%3], %1, 0" : "=&v"(c) : "v"(b), "n"(R), "n"(R + 3)); }
DEV void mfence5(f32x4 (&c)[5]) { asm volatile("s_nop 7" : "+v"(c[0]), "+v"(c[1]), "+v"(c[2]), "+v"(c[3]), "+v"(c[4])); }
DEV void mfence7(f32x4 (&c)[7]) { asm volatile("s_nop 7" : "+v"(c[0]), "+v"(c[1]), "+v"(c[2]), "+v"(c[3]), "+v"(c[4]), "+v"(c[5]), "+v"(c[6])); }

DEV u32x4 lds16(u32 a) { return *(const LAS u32x4*)(size_t)a; }
DEV u32x2 lds8(u32 a) { return *(const LAS u32x2*)(size_t)a; }
DEV void sts16(u32 a, u32x4 v) { *(LAS u32x4*)(size_t)a = v; }
DEV void sts8(u32 a, u32x2 v) { *(LAS u32x2*)(size_t)a = v; }
DEV void sts4(u32 a, float v) { *(LAS float*)(size_t)a = v; }
DEV u32x4 tr16(u32 lo, u32 hi) {
  const u32x2 a = __builtin_bit_cast(u32x2, __builtin_amdgcn_ds_read_tr16_b64_v4i16((LAS s16x4*)(size_t)lo));
  const u32x2 b = __builtin_bit_cast(u32x2, __builtin_amdgcn_ds_read_tr16_b64_v4i16((LAS s16x4*)(size_t)hi));
  return u32x4{a[0], a[1], b[0], b[1]};
}
DEV u32 pk(float lo, float hi) { f32x2 v = {lo, hi}; return __builtin_bit_cast(u32, __builtin_convertvector(v, bf16x2)); }
DEV float blo(u32 u) { return __uint_as_float(u << 16); }
DEV float bhi(u32 u) { return __uint_as_float(u & 0xffff0000u); }
template <int CTRL> DEV float dppadd(float v) {
  return v + __uint_as_float(__builtin_amdgcn_update_dpp(0u, __float_as_uint(v), CTRL, 0xf, 0xf, true));
}
DEV float sum16(float v) { v = dppadd<0x128>(v); v = dppadd<0x124>(v); v = dppadd<0x122>(v); v = dppadd<0x121>(v); return v; }
DEV float addx16(float v) { u32 u = __float_as_uint(v); auto r = __builtin_amdgcn_permlane16_swap(u, u, false, false); return __uint_as_float(r[0]) + __uint_as_float(r[1]); }
DEV float addx32(float v) { u32 u = __float_as_uint(v); auto r = __builtin_amdgcn_permlane32_swap(u, u, false, false); return __uint_as_float(r[0]) + __uint_as_float(r[1]); }
DEV u32 sel(u32 c, u32 a0, u32 a1, u32 a2) { return c == 0 ? a0 : c == 1 ? a1 : a2; }
DEV int blk5(int j, int i) { int v = i + (j & 1); return 5 * j + (v >= 5 ? v - 5 : v); }

DEV void publish(rsrc_t rws, u32 soff, u32x2 tag) {
  asm volatile("s_mov_b64 exec, 1\n\tbuffer_store_dwordx2 %0, off, %1, %2 sc1\n\ts_mov_b64 exec, -1" ::"v"(tag), "s"(rws), "s"(soff) : "memory");
}
template <u64 MASK = ~0ull, bool LAST = false> DEV void settle(u32x2& v, rsrc_t rws, u32 voff, u32 soff, u32x2 tag) {
  const u64 tg = __builtin_bit_cast(u64, tag);
  landed(v);
  if (__builtin_expect((__builtin_amdgcn_uicmpl(__builtin_bit_cast(u64, v), tg, 33) & MASK) != 0, 0)) {
    if constexpr (LAST) asm volatile(".Lfnet_end:");
#pragma nounroll
    for (u32 spin = 0; spin < SPIN_MAX; ++spin) {
      ldv8<1, 0>(v, voff, rws, soff);
      waitv<0>();
      landed(v);
      if ((__builtin_amdgcn_uicmpl(__builtin_bit_cast(u64, v), tg, 33) & MASK) == 0) break;
    }
  }
}
DEV void settle_here(u32x2& v, rsrc_t rws, u32 voff, u32 soff, u32x2 tag) {
  u32 spin;
  asm volatile("v_cmp_ne_u64 vcc, %2, %0\n\ts_cbranch_vccz .Lset%=\n\ts_mov_b32 %1, %6\n"
               ".Lpoll%=:\n\tbuffer_load_dwordx2 %0, %3, %4, %5 offen sc1\n\ts_waitcnt vmcnt(0)\n\tv_cmp_ne_u64 vcc, %2, %0\n\t"
               "s_sub_u32 %1, %1, 1\n\ts_cbranch_scc1 .Lset%=\n\ts_cbranch_vccnz .Lpoll%=\n.Lset%=:"
               : "+v"(v), "=&s"(spin) : "s"(__builtin_bit_cast(u64, tag)), "v"(voff), "s"(rws), "s"(soff), "n"(SPIN_MAX) : "vcc", "scc", "memory");
}
template <int P0, int N1, int N2, int N3, int BAR, int SB0, class Pf, class Ex>
DEV void handoff(rsrc_t rws, u32 mine, u32 voff, u32 soff, u32x2 tag, Pf&& pf, Ex&& extra) {
  if constexpr (BAR & 2) bar();
  sfor<N1>([&](auto I_) { pf(std::integral_constant<int, P0 + I_.value>{}); brk(); });
  waitv<N1>();
  publish(rws, mine, tag);
  if constexpr (BAR & 1) bar();
  extra();
  sfor<N2>([&](auto I_) { pf(std::integral_constant<int, P0 + N1 + I_.value>{}); brk(); });
  u32x2 v;
  if constexpr (BAR & 4) bar();
  ldv8<1, 0>(v, voff, rws, soff);
  sfor<N3>([&](auto I_) { brk(); pf(std::integral_constant<int, P0 + N1 + N2 + I_.value>{}); });
  waitv<N3>();
  settle_here(v, rws, voff, soff, tag);
}

constexpr int ubid(int u) { return NB == 4 ? u & 3 : u == 15 ? 3 : u % 6 < 3 ? u % 6 : u % 6 == 5 ? 3 : u % 6 + 1; }
constexpr int unext(int u) { for (int v = u + 1; v < 16; ++v) if (ubid(v) == ubid(u)) return v; return -1; }
constexpr int p0pos(int kind, int id) {
  int n = -(4 * NB + 3 + EP0 + EP1 + EP2 + (TB == 1) + (TW > 0)), e = 0, x = 0;
  for (int u = 0; u <= NB; ++u) {
    if (u == U0) { for (int i = 0; i < EP0; ++i) { ++n; if (kind == 1 && id == e) return n; ++e; } n += 3; }
    if (u == U1) n += (TB == 1) + (TW > 0);
    if (u == U1) for (int i = 0; i < EP1; ++i) { ++n; if (kind == 1 && id == e) return n; ++e; }
    if (u == NB) for (int i = 0; i < EP2; ++i) { ++n; if (kind == 1 && id == e) return n; ++e; }
    if (u < NB) { n += 4; if (kind == 0 && id == u) return n; }
  }
  for (int u = 0; u < 16; ++u) {
    if (u == UP) { ++n; if (kind == 3) return n; }
    if (kind == 4 && id == u) return n;
    if (unext(u) >= 0) { n += 4; if (kind == 0 && id == unext(u)) return n; }
    for (int i = 0; i < NE && e < NPC; ++i) { ++n; if (kind == 1 && id == e) return n; ++e; }
    if (kind == 5 && id == u) return n;
    if (u >= UX) for (int i = 0; i < NX && x < 10; ++i) { ++n; if (kind == 2 && id == x) return n; ++x; }
  }
  return kind == 4 ? n : 1000;
}
constexpr int ksops(int g, int N, int total) {
  int o = PRO;
  for (int i = 0; i < g; ++i) {
    int c = o < N ? (o + 2 <= i + RING ? 2 : 1) : TAILR;
    if (o < N && o + c > N && TAILR < 2) c = N - o;
    o = o + c < total ? o + c : total;
  }
  return o;
}
constexpr int kstail(int N) { return ksops(N, N, 1000) - N; }
struct G3Plan { int blk[147], ks[147], vop[147], wait[147], e0[148], e1[147], npf, nst; };
constexpr G3Plan g3plan() {
  G3Plan P = {};
  constexpr int L = 20 - K1, NA = 7 * (K1 + 1), gend[4] = {NA + 2 * L, NA + 4 * L, NA + 5 * L, NA + 7 * L}, eend[3] = {32, 64, 80};
  for (int n = 0; n < 147; ++n) {
    const int r = n - NA;
    if (r < 0) { const int k = n / 7; P.blk[n] = n % 7; P.ks[n] = k < KX ? k : k == KX ? 20 : k - 1; }
    else if (r < 4 * L) { P.blk[n] = 2 * (r / (2 * L)) + (r & 1); P.ks[n] = K1 + (r % (2 * L)) / 2; }
    else if (r < 5 * L) { P.blk[n] = 6; P.ks[n] = K1 + r - 4 * L; }
    else { P.blk[n] = 4 + ((r - 5 * L) & 1); P.ks[n] = K1 + (r - 5 * L) / 2; }
  }
  int hpos[40] = {}, pos = 2 * R3, nh = 2 * R3, e = 0, seen = -1;
  for (int i = 0; i < 2 * R3; ++i) hpos[i] = i + 1;
  for (int n = 0; n < 147; ++n) {
    P.wait[n] = -1;
    if (P.ks[n] < 20 && P.ks[n] > seen) { seen = P.ks[n]; P.wait[n] = 2 * seen + 1 < nh ? pos - hpos[2 * seen + 1] : -1000; }
    int lim = 0;
    for (int g = 0; g < 3; ++g) if (n >= gend[g] + 2) lim = eend[g];
    P.e0[n] = e;
    if (e < lim) ++e;
    P.e1[n] = e;
    P.vop[n] = -1;
    if (n < NA ? (n % 7) % 2 == 1 : n % 2 == 0) {
      const int need = P.nst < 4 ? 16 * (P.nst + 1) : 64 + 8 * (P.nst - 3);
      if (nh < 40) { P.vop[n] = nh; hpos[nh++] = ++pos; }
      else if (P.nst < 6 && e >= need) { P.vop[n] = 200 + P.nst++; ++pos; }
      else if (P.npf < NPF3) { P.vop[n] = 100 + P.npf++; ++pos; }
    }
    const int cap = 2 - (n % 2) - (P.vop[n] >= 0);
    for (int c = 0; c < cap && e < lim; ++c) ++e;
  }
  P.e0[147] = e;
  return P;
}
constexpr G3Plan G3 = g3plan();
template <int O> DEV void statop(const u32x4& st, float (&v)[4]) {
  if constexpr (O < 2) asm volatile("v_add_f32 %0, %1, %2" : "=v"(v[O]) : "v"(st[O]), "v"(st[O + 2]));
  else if constexpr (O < 14) {
    constexpr int c = O & 1, ph = ((O - 2) % 6) >> 1;
    if constexpr (ph == 0) asm volatile("v_mov_b32 %0, %1" : "=v"(v[2 + c]) : "v"(v[c]));
    else if constexpr (ph == 1 && O < 8) asm volatile("v_permlane16_swap_b32 %0, %1" : "+v"(v[c]), "+v"(v[2 + c]));
    else if constexpr (ph == 1) asm volatile("v_permlane32_swap_b32 %0, %1" : "+v"(v[c]), "+v"(v[2 + c]));
    else vadd(v[c], v[2 + c]);
  }
  else if constexpr (O < 16) asm volatile("v_mul_f32 %0, 0x3acccccd, %0" : "+v"(v[O - 14]));
  else if constexpr (O == 16) asm volatile("v_fma_f32 %0, -%1, %1, %0" : "+v"(v[1]) : "v"(v[0]));
  else if constexpr (O == 17) asm volatile("v_max_f32 %0, 0, %0" : "+v"(v[1]));
  else asm volatile("v_add_f32 %0, 0x3727c5ac, %0" : "+v"(v[1]));
}
template <int NK, int PITCH, int NT, int NPRE, int SB0, class Tail, class Pre, class Post>
DEV void ksplit(f32x4 (&res)[5], const u32x4 (&wv)[10], const u32 (&vk)[12], rsrc_t rws, u32 sA, u32 vlast, u32 l0, int w, int lane, Tail&& tail, Pre&& pre, Post&& post) {
  constexpr int N = 4 * NK, KB = (5 + WSP - 1) / WSP + 1;
  u32x4 a[RING];
  f32x4 acc0[5], part[5];
  const u32 v0 = lane * 16;
  u32 base[4];
#pragma unroll
  for (int s = 0; s < 4; ++s) base[s] = sA + ((w + 1 + s) & 3) * (PITCH * 1024);
  auto loadA = [&](auto N_) {
    constexpr int n = N_.value, s = n / NK, k = n % NK;
    if constexpr (NK == 14 && k < 12) ldv<1, 0>(a[n % RING], vk[k], rws, base[s]);
    else ldv<1, (k & 3) * 1024>(a[n % RING], k == 13 ? vlast : v0, rws, base[s] + (k >> 2) * 4096);
  };
  sfor<PRO>([&](auto N_) { loadA(N_); brk(); });
  u32 wr = l0 + L_RED + w * 5120 + v0, rd2 = l0 + L_RED + ((w + 2) & 3) * 5120 + v0, rd1 = l0 + L_RED + ((w + 1) & 3) * 5120 + v0;
  landed(wr); landed(rd2); landed(rd1);
  SB;
  sfor<N>([&](auto N_) {
    constexpr int n = N_.value, s = n / NK, k = n % NK;
    constexpr int total = N + NT, o0 = ksops(n, N, total), o1 = ksops(n + 1, N, total);
    f32x4(&cur)[5] = (s & 1) ? res : acc0;
    f32x4(&old)[5] = (s & 1) ? acc0 : res;
    waitv<o0 - n - 1>();
    if constexpr (k == 0 && s >= 2) waitl0();
    if constexpr ((BL & 2) && NK == 14 && n == NK - 2) bar();
    sfor<5>([&](auto I_) {
      constexpr int i = I_.value;
      if constexpr (k == 0 && s < 2) mfA0<4 * (5 * k + i)>(cur[i], a[n % RING]);
      else if constexpr (k < 12) mfA<4 * (5 * k + i)>(cur[i], a[n % RING]);
      else mf(cur[i], wv[5 * (k - 12) + i], a[n % RING]);
      SB;
      constexpr bool vm = (i == 1 || i == 3) && o0 + (i >> 1) < o1;
      if constexpr (vm) { constexpr int op = o0 + (i >> 1); if constexpr (op < N) loadA(std::integral_constant<int, op>{}); else tail(std::integral_constant<int, op - N>{}); }
      constexpr int wi = (i & 1) == 0 && i / 2 < WSP ? WSP * (k - 1) + i / 2 : -1;
      if constexpr (s > 0 && k >= 1 && wi >= 0 && wi < 5) dsw<((s - 1) & 1) * 20480 + wi * 1024>(wr, old[wi]);
      if constexpr (s > 0 && k == KB) {
        if constexpr (i == 0) { waitl0(); bar(); }
        if constexpr (s < 3) dsrf<((s - 1) & 1) * 20480 + i * 1024>(old[i], rd2); else dsrf<i * 1024>(part[i], rd1);
      }
      constexpr int pi = 5 * (n - NK) + i;
      if constexpr (pi >= 0 && pi < NPRE) pre(std::integral_constant<int, pi>{});
      if constexpr (s == 3 && k > KB) {
        constexpr int m = 5 * (k - KB - 1) + i - 2, per = (NK - KB - 1) * 5 - 2 >= 20 ? 1 : 2;
        if constexpr (m == 0) { waitl0(); landed(part[0]); landed(part[1]); landed(part[2]); landed(part[3]); landed(part[4]); }
        if constexpr (m >= 0) sfor<per>([&](auto J_) { constexpr int c = per * m + J_.value; if constexpr (c < 20) post(std::integral_constant<int, c>{}, part[c >> 2][c & 3]); });
      }
      SB;
    });
  });
  static_assert(NPRE <= 5 * (2 * NK + KB) && ((NK - KB - 1) * 5 - 2) * 2 >= 20, "shadow operations fit");
  mfence5(res);
}

__global__ __launch_bounds__(256, 1) void fnet_block(Params p) {
  extern __shared__ __attribute__((aligned(16))) char smem[];
  asm volatile("" ::: "a0", "a63", "a127", "a191", "a255");
  u32 tch = 0;
  if constexpr (CPF == 2) {
    u64 pc;
    asm volatile("s_getpc_b64 %0\n.Lfnet_pc:" : "=s"(pc));
    u32 o = ((threadIdx.x >> 6) * 5 + min(threadIdx.x & 63, 4u)) * 128 + (blockIdx.x >> 3) * 2560;
    asm volatile("v_min_u32 %1, .Lfnet_end-.Lfnet_pc+48, %1\n\tglobal_load_dword %0, %1, %2" : "=v"(tch), "+v"(o) : "s"(pc) : "memory");
  }
  const int b = blockIdx.x, w = __builtin_amdgcn_readfirstlane(threadIdx.x >> 6), lane = threadIdx.x & 63;
  const int l = b >> 3, sg = (b & 7) * 2 + (l >> 4), mi = l & 15, rg = sg * 2 + (mi >> 3), j = mi & 7, gid = sg * 16 + mi;
  const int sg3 = REMAP == 2 ? (b & 4) * 2 + (l >> 2) : REMAP ? (b & 6) * 2 + (l >> 3) : sg, mi3 = REMAP == 2 ? (b & 3) * 4 + (l & 3) : REMAP ? (b & 1) * 8 + (l & 7) : mi;
  const int rg1 = REMAP1 == 1 ? (b & 6) * 4 + (l >> 2) : REMAP1 == 2 ? (b & 4) * 4 + (l >> 1) : rg, j1 = REMAP1 == 1 ? (b & 1) * 4 + (l & 3) : REMAP1 == 2 ? (b & 3) * 2 + (l & 1) : j;
  const int t = lane & 15, q = lane >> 4;
  auto vlines = [&](int n) { return lane < n ? (u32)lane * 128 : 0x7f000000u; };
  const u32 l0 = (u32)(size_t)(const LAS char*)smem, v0 = lane * 16;
  const rsrc_t rws = mkrs(p.ws, WS_END), rx = mkrs(p.x, S * D * 2), rcr = mkrs(p.cr, S * S * 2), rci = mkrs(p.ci, S * S * 2);
  u64 non;
  asm volatile("s_load_dwordx2 %0, %1, %2" : "=s"(non) : "s"(p.ws), "s"(O_NON) : "memory");

  u32x2 xv[5];
  sfor<5>([&](auto C_) { ldv8<2 * NTXX, 256 * C_.value>(xv[C_.value], (lane >> 5) * 1280 + (lane & 31) * 8, rx, (u32)(8 * b + 2 * w) * 1280); });
  if constexpr (CPF == 1) {
    u64 pc; u32 len;
    asm volatile("s_getpc_b64 %0\n.Lfnet_pc:\n\ts_mov_b32 %1, .Lfnet_end-.Lfnet_pc+52" : "=s"(pc), "=s"(len));
    touch(tch, vlines(5) + (l * 4 + w) * 640, mkrs((const void*)pc, len), 0);
  }
  int rot = ROT, crot = rot;
  u32 va[4], ra[4];
#pragma unroll
  for (int i = 0; i < 4; ++i) {
    const u32 row = 4 * i + q;
    va[i] = (u32)(64 * rg + 16 * w + row) * 4096 + ((t ^ row) << 4) - i * 1024;
    ra[i] = l0 + w * 4096 + t * 256 + (((4 * i + q) ^ t) << 4);
  }
  const u32 mB = l0 + L_XB + w * 5120, mA = l0 + L_CR + w * 4096;
  auto ubuf = [](int u) { const int i = ubid(u); return i >= 4 ? (i & 1) * 20480 : (i & 2) ? 131072 + (i & 1) * 16384 : 32768 + (i & 1) * 16384; };
  static_assert(L_WT + 24576 == 131072 && L_LN == 147456, "spare buffers");
  auto unit = [&](auto U_) {
    constexpr int u = U_.value;
    setm0<ubuf(u)>(ubid(u) >= 4 ? mB : mA);
    const u32 so = ((rot + u) & 15) * 256;
    brk();
    sfor<4>([&](auto N_) { dmago<(u < NTI ? 2 : 0), 1024 * N_.value>(va[N_.value], rci, so); brk(); });
  };
  const u32 vI = (u32)(64 * rg + 8 * j + 2 * w + (lane >> 5)) * 4096 + (lane & 31) * 128;
  sfor<UA>(unit);
  if constexpr (TI == 1) touch0(tch, vI, rci, 0);
  auto crpiece = [&](auto C_) { constexpr int c = C_.value; setm0<(c >> 2) * 16384>(mA); brk(); dmago<2 * NTR, 1024 * (c & 3)>(va[c & 3], rcr, ((crot + (c >> 2)) & 15) * 256); };
  static_assert(EPA <= EP0 && (EPA == 0 || (PORD && TA <= 2 && EPA <= 8)), "early pieces");
  sfor<EPA>([&](auto C_) { crpiece(C_); brk(); });
  SB;
  u32 g = b * 4 + w;
  asm volatile("" : "+s"(g));
  const u32 ty = (g >= 378) + (g >= 738);
  const u32 gl = g - sel(ty, 0, 378, 738), gd = (gl * sel(ty, 1214, 3277, 9363)) >> 16, gm = gl - gd * sel(ty, 54, 20, 7);
  const u32 gp = ty < 2 ? gm : gd, kg = ty < 2 ? gd : gm;
  const bool tshort = ty != 1 && kg == 6;
  const u32 lwt = l0 + L_WT + w * 6144;
  const u32 pitch = ty ? 1280 : 3456, nrec = ty < 2 ? D * F * 2 : D * D * 2;
  const void* pa = ty == 0 ? p.w1 : ty == 1 ? p.w2 : p.dr;
  const rsrc_t rw0 = mkrs(pa, nrec), rw1 = mkrs(ty < 2 ? pa : p.di, nrec);
  u32 vw[3], vb[5];
  const u32 vhi = ty < 2 ? 48 * pitch : 0;
#pragma unroll
  for (int i = 0; i < 3; ++i) {
    const u32 o = i * 64 + lane, row = (o * 5462) >> 16, c = o - row * 12;
    u32 va_ = (96 * kg + 16 * i + (lane >> 2)) * pitch + gp * 64 + (lane & 3) * 16;
    u32 vd_ = (32 * (gp >> 1) + 4 * (gp & 1) + 8 * (row >> 2) + (row & 3)) * 1280 + kg * 192 + ((c ^ (row >> 2)) << 4);
    landed(va_); landed(vd_);
    vw[i] = ty < 2 ? va_ : vd_;
    landed(vw[i]);
  }
#pragma unroll
  for (int i = 0; i < 5; ++i) { const u32 o = (u32)(w * 5 + i) * 64 + lane, sb = (o * 3277) >> 18; vb[i] = O_XNT + j * 1280 + sb * 10240 + (o - sb * 80) * 16 - (i & 3) * 1024; landed(vb[i]); }
  u32 sR[6];
  {
    const u32 sR0 = sel(ty, O_W1P, O_W2P, O_DP) + (gp * sel(ty, 40, 108, 40) + kg * sel(ty, 3, 3, 6)) * 1024, sRa = sel(ty, 20480, 55296, 1024), sRb = sel(ty, 1024, 1024, 2048);
#pragma unroll
    for (int f = 0; f < 6; ++f) { sR[f] = sR0 + (f & 1) * sRa + (f >> 1) * sRb; asm volatile("" : "+s"(sR[f])); }
  }
  u32 vRa = g < 1018 ? v0 : 0x7f000000u, vRb = tshort ? 0x7f000000u : vRa, vRc = ty == 0 && lane < 32 ? (u32)(gp * 32 + lane) * 32 + kg * 4 : 0x7f000000u;
  u32 vf0 = (16 * ((rot + (lane >> 2)) & 15) + 4 * w + (lane & 3)) * FS0;
  landed(vRa); landed(vRb); landed(vRc); landed(vf0);
  const u32 vT = (u32)(64 * rg + 8 * j + 2 * w + ((lane >> 1) & 1)) * 4096 + (lane & 1) * 128;
  u32 vT1 = lane < 4 ? vT : 0x7f000000u;
  landed(vT1);
  auto piece = [&](auto E_) {
    constexpr int e = PORD && E_.value < NPC ? (E_.value + 6) % NPC : E_.value;
    if constexpr (e < 6) dma<2 * NTW, e * 1024>(lwt, e < 3 ? vw[e % 3] : vw[e % 3] + vhi, e < 3 ? rw0 : rw1, 0);
    else if constexpr (e < NPC - 8) touch0(tch, lane < 4 * (TA - 2) ? vT + (lane >> 2) * 256 : 0x7f000000u, rcr, 512);
    else if constexpr (e < NPC) crpiece(std::integral_constant<int, e - (NPC - 8)>{});
    brk();
  };
  SB;
  u32 vTW = 0, vTW2 = 0;
  if constexpr (TW) {
    const u32 tb = g >> 3, ln = (g & 7) * 128;
    u32 a0 = O_F + (tb * 54 + 7 * lane + 3) * 1024 + ln, a1 = O_H + (tb * 20 + 5 * lane - 28) * 1024 + ln;
    u32 a2 = ((lane & 2) ? O_FW : O_CS) + g * 128 + (lane & 1) * (O_ST - O_CS), lim = (lane & 2) ? 768 : (lane & 1) ? 1024 : 432;
    landed(a0); landed(a1); landed(a2); landed(lim);
    a0 = lane >= 6 ? a1 : a0;
    landed(a0);
    a0 = lane >= 10 ? a2 : a0;
    landed(a0);
    a0 = lane >= 14 || lane == 11 ? 0x7f000000u : a0;
    landed(a0);
    a0 = lane >= 10 && g >= lim ? 0x7f000000u : a0;
    landed(a0);
    const bool late = (TW2 >> (lane & 15)) & 1 || (TW2 && lane == 10 && g >= 256);
    vTW = late ? 0x7f000000u : a0;
    vTW2 = late ? a0 : 0x7f000000u;
    landed(vTW);
    if constexpr (TW2) landed(vTW2);
  }
  u32 vTB = vlines(w < 2 ? 27 : 0);
  landed(vTB);
  const void* pTB = w ? p.b1 : p.b2;
  asm volatile("" : "+s"(pTB));
  const rsrc_t rTB = mkrs(pTB, w ? F * 2 : D * 2);
  SB;
  sfor<EP0 - EPA>([&](auto I_) { piece(std::integral_constant<int, EPA + I_.value>{}); });
  waitv<(CPF == 1) + 4 * UA + (TI == 1) + EP0>();
  if constexpr (UB > 0) unit(std::integral_constant<int, UA>{});
  {
    f32x2 f[10], s = {0.f, 0.f};
#pragma unroll
    for (int c = 0; c < 5; ++c) {
      landed(xv[c]);
      f[2 * c] = f32x2{blo(xv[c][0]), bhi(xv[c][0])}; f[2 * c + 1] = f32x2{blo(xv[c][1]), bhi(xv[c][1])};
      s += f[2 * c] + f[2 * c + 1];
    }
    const float mean = addx16(sum16(s[0] + s[1])) * (1.f / D);
    f32x2 q2 = {0.f, 0.f};
    SB;
    if constexpr (UB > 1) unit(std::integral_constant<int, UA + 1>{});
#pragma unroll
    for (int c = 0; c < 10; ++c) { f[c] -= mean; q2 += f[c] * f[c]; }
    const float rs = __builtin_amdgcn_rsqf(addx16(sum16(q2[0] + q2[1])) * (1.f / D) + 1e-5f);
    SB;
    const u32 dst = l0 + L_LN + (2 * w + (lane >> 5)) * 1280 + (lane & 31) * 8;
#pragma unroll
    for (int c = 0; c < 5; ++c) { const f32x2 u = f[2 * c] * rs, v = f[2 * c + 1] * rs; sts8(dst + c * 256, u32x2{pk(u[0], u[1]), pk(v[0], v[1])}); }
  }
  asm volatile("s_waitcnt lgkmcnt(0)" : "+s"(non)::"memory");
  non += 0x9E3779B97F4A7C15ull;
  const u32x2 tag = {(u32)non, (u32)(non >> 32)};
  bar();
  {
    const u32 a = l0 + L_LN + (t >> 2) * 1280 + q * 32 + (t & 3) * 8 + w * 128;
#pragma unroll
    for (int i = 0; i < 3; ++i) st16<SC1>(tr16(a + i * 512, a + i * 512 + 4 * 1280), rws, i == 2 && w >= 2 ? 0x7f000000u : v0, O_XNT + b * 10240 + (w + 4 * i) * 1024);
  }
  SB;
  static_assert(U0 <= U1 && U1 < NB && (NB == 4 || (UX >= 9 && UX + 5 / NX >= 10)) && UX >= UP && p0pos(1, NPC - 1) < 1000 && p0pos(2, 9) < 1000 && RT < 16 && p0pos(0, RT) > p0pos(1, PORD ? NPC - 1 : 5) && UB == 0, "unit order");
  if constexpr (TI == 2) touch0(tch, vI, rci, 0);
  sfor<U1 - U0>([&](auto I_) { unit(std::integral_constant<int, U0 + I_.value>{}); });
  SB;
  if constexpr (TW) touch(tch, vTW, rws, 0);
  if constexpr (TB == 1) touch0(tch, vTB, rTB, 0);
  sfor<EP1>([&](auto I_) { piece(std::integral_constant<int, EP0 + I_.value>{}); });
  SB;
  waitv<4 * (U1 - U0) + (TI == 2) + (TB == 1) + (TW > 0) + EP1>();
  bar();
  if (w == 0) publish(rws, O_FL + b * FS0, tag);
  sfor<NB - U1>([&](auto I_) { unit(std::integral_constant<int, U1 + I_.value>{}); });
  sfor<EP2>([&](auto I_) { piece(std::integral_constant<int, EP0 + EP1 + I_.value>{}); });
  u32x4 rt[6];
  u32 csw = 0;
  auto retile = [&] {
  if (ty == 0) {
    const u32 a = lwt + (8 * q + (t >> 2)) * 64 + (t & 3) * 8;
    float cs[2] = {0.f, 0.f};
#pragma unroll
    for (int f = 0; f < 6; ++f) {
      rt[f] = tr16(a + (f & 1) * 32 + (f >> 1) * 2048, a + (f & 1) * 32 + (f >> 1) * 2048 + 256);
      float e = 0.f;
#pragma unroll
      for (int i = 0; i < 4; ++i) e += blo(rt[f][i]) + bhi(rt[f][i]);
      cs[f & 1] += f >= 4 && tshort ? 0.f : e;
    }
    const float c0 = addx32(addx16(cs[0])), c1 = addx32(addx16(cs[1]));
    csw = __float_as_uint(q ? c1 : c0);
  } else if (ty == 1) {
    const u32 a = lwt + (4 * q + (t >> 2)) * 64 + (t & 3) * 16;
#pragma unroll
    for (int f = 0; f < 6; ++f) {
      rt[f] = tr16(a + (f & 1) * 8 + (f >> 1) * 2048, a + (f & 1) * 8 + (f >> 1) * 2048 + 1024);
    }
  } else {
#pragma unroll
    for (int f = 0; f < 6; ++f) {
      const u32 a = lwt + t * 192 + (((2 * f + (q >> 1)) ^ (t >> 2)) << 4) + (q & 1) * 8;
      const u32x2 re = lds8(a), im = lds8(a + 3072);
      rt[f] = u32x4{re[0], re[1], im[0] ^ 0x80008000u, im[1] ^ 0x80008000u};
    }
  }
#pragma unroll
  for (int f = 0; f < 6; ++f) landed(rt[f]);
  };
  auto store_rt = [&](auto F_) {
    constexpr int f = F_.value;
    if constexpr (f < 6) sta16<0>(rt[f], f < 4 ? vRa : vRb, rws, sR[f]);
    else if constexpr (f == 6) sta4(csw, vRc, rws, O_CS);
  };
  SB;
  u32x2 flv;
  const u32 vf1 = (64 * w + lane) * FS0;
  sfor<16>([&](auto U_) {
    constexpr int u = U_.value;
    if constexpr (u == UP) ldv8<1, 0>(flv, vf0, rws, O_FL);
    waitv<p0pos(4, u) - p0pos(0, u)>();
    sfor<4>([&](auto K_) { constexpr int k = K_.value; dsra<16 * u + 4 * k, ubuf(u) & 65535>(ra[k] + (ubid(u) >= 4 ? L_XB + w * 1024 : ubuf(u) & ~65535)); });
    waitl0();
    if constexpr (unext(u) >= 0) unit(std::integral_constant<int, unext(u)>{});
    sfor<NE>([&](auto I_) { piece(std::integral_constant<int, EP0 + EP1 + EP2 + NE * u + I_.value>{}); });
    if constexpr (u == RT) { retile(); SB; }
    if constexpr (u == UX) {
      if constexpr (p0pos(0, u) < p0pos(3, 0)) waitv<p0pos(5, u) - p0pos(3, 0)>();
      settle<0xffff>(flv, rws, vf0, O_FL, tag);
    }
    if constexpr (u >= UX) sfor<NX>([&](auto I_) {
      constexpr int x = NX * (u - UX) + I_.value, it = x / 5, n = x % 5;
      if constexpr (x < 10) { setm0<it * 20480 + (n == 4 ? 4096 : 0)>(mB); brk(); dmago<1, 1024 * (n & 3)>(vb[n], rws, ((rot + it) & 15) * 163840); brk(); }
    });
  });
  if constexpr (STG == 0) sfor<7>([&](auto F_) { store_rt(F_); brk(); });
  asm volatile("" : "+s"(rot));

  f32x4 aP[5], aQ[5];
  u32x4 fr[2][6];
  const u32 rb0 = l0 + L_XB + q * 1280 + t * 16, rb1 = rb0 + 40960;
  u32 sD[5];
#pragma unroll
  for (int i = 0; i < 5; ++i) sD[i] = O_DP + (blk5(j1, i) * 40 + 10 * w) * 1024;
  u32 vk4[12], vkt, vkr = v0 + ROT4 * 1024;
  auto load_d = [&](auto N_) {
    constexpr int n = N_.value, k = n / 5, i = n % 5;
    lda<4 * n, 1, (k & 3) * 1024>(v0, rws, sD[i] + (k >> 2) * 4096);
  };
  u32 soA = ((rot + LA) & 15) * 256, soB = soA * 640;
  constexpr int SL = STG ? (7 + STG - 1) / (STG ? STG : 1) - 1 : 0;
  auto cnt = [](int it) { return it < 0 ? p0pos(4, 16) - (p0pos(1, NPC - 5 - 6 * PORD) > p0pos(2, 4) ? p0pos(1, NPC - 5 - 6 * PORD) : p0pos(2, 4)) : 9 + (it >= XS ? XD : 0) + 3 * (it > 0 && (it == SX || it == SB2 || it == SCS)) + (TA > 0 && it + TA < 16) + (it == FL1) + (STG == 0 ? 0 : it < SL ? STG : it == SL ? 7 - SL * STG : 0); };
  const int nb0 = 7 * mi3 - max(mi3 - 12, 0), odd = (0x0aaa >> mi3) & 1;
  const int kp5 = (5 * j1 + 1) >> 1;
  const u32 cfull = kp5 * 64, chalf = (blk5(j1, 4) >> 1) * 64 + (j1 & 1) * 8;
  u32x4 x01, x23, b01, b23, csv, csu;
  u32x2 x4, b4;
  u32 b1v;
  const int tid = w * 64 + lane;
  u32 vx = (u32)t * 1280 + q * 16, vq = q * 16, vcol;
  {
    const int i = tid < 112 ? tid >> 4 : 6;
    int v = i + odd; v = v >= 7 ? 0 : v; v = (mi3 >= 12 && i == 6) ? 0 : v;
    vcol = (u32)(nb0 + v) * 16 + (tid & 15);
  }
  u32 vcs = vcol * 32, vb1 = vcol * 2;
  landed(vx); landed(vq); landed(vcs); landed(vb1);
  const u32 sx = (u32)(64 * rg1 + 16 * w) * 1280;
  const rsrc_t rbi1 = mkrs(p.b1, F * 2), rb2 = mkrs(p.b2, D * 2);
  auto small = [&](auto N_) {
    constexpr int n = N_.value;
    if constexpr (n == 0) ldv<0, 0>(x01, vx, rx, sx + cfull);
    else if constexpr (n == 1) ldv<0, 64>(x23, vx, rx, sx + cfull);
    else if constexpr (n == 2) ldv8<0, 0>(x4, vx, rx, sx + chalf);
    else if constexpr (n == 3) ldv<1, 0>(csv, vcs, rws, O_CS);
    else if constexpr (n == 4) ldv<1, 16>(csu, vcs, rws, O_CS);
    else if constexpr (n == 5) ldu16(b1v, vb1, rbi1);
    else if constexpr (n == 6) ldv<0, 0>(b01, vq, rb2, cfull);
    else if constexpr (n == 7) ldv<0, 64>(b23, vq, rb2, cfull);
    else ldv8<0, 0>(b4, vq, rb2, chalf);
  };
  constexpr int RM = LA == 2 ? 0x175 : 0x03f;
  constexpr u64 VM = LA == 2 ? 0x208822088aull : 0xd014050140ull;
  auto below = [](u64 m, int n) { int c = 0; for (int i = 0; i < n; ++i) c += (m >> i) & 1; return c; };
  sfor<16>([&](auto IT_) {
    constexpr int it = IT_.value;
    waitv<cnt(it - 1) + (STG == 0 && it < 2 ? 7 : 0)>();
    if constexpr (it == 2) settle(flv, rws, vf0, O_FL, tag);
    if constexpr (it == FL1 + 2) settle<~0ull, CPF && CEND>(flv, rws, vf1, O_FL + 256 * FS0, tag);
    if constexpr (LA == 3) waitl0();
    bar();
    if constexpr (it == SL + 2) { if (w == 0) publish(rws, O_FL + (256 + b) * FS0, tag); }
    if constexpr (it == 2) { if (w == 0 && b == 0) publish(rws, O_NON, tag); }
    SB;
    sfor<4>([&](auto K_) {
      constexpr int K = K_.value, U = (K + 1) & 1, G = 4 * it + K - 1;
      sfor<10>([&](auto G_) {
        constexpr int g = G_.value, i = g >> 1, m = 10 * K + g;
        if constexpr (G == 0) { if constexpr (g & 1) mfB0<4 * G>(aQ[i], fr[U][1 + i]); else mf0(aP[i], fr[U][1 + i], fr[U][0]); }
        else if constexpr (G > 0) { if constexpr (g & 1) mfB<4 * G>(aQ[i], fr[U][1 + i]); else mf(aP[i], fr[U][1 + i], fr[U][0]); }
        else brk();
        if constexpr ((RM >> g) & 1) {
          constexpr int ri = below(RM, g), r = ri == 0 ? 1 : ri == 1 ? 0 : ri;
          if constexpr (r == 0) dsr<(it & 3) * 16384>(fr[U ^ 1][0], ra[K]);
          else dsr<(it & 1) * 20480 + K * 5120 + (r - 1) * 256>(fr[U ^ 1][r], (it & 2) ? rb1 : rb0);
        }
        if constexpr (LA == 2) { if constexpr (g == 5) waitl<4>(); if constexpr (g == 9) waitl<2>(); }
        else if constexpr (g == 9) waitl0();
        constexpr int n = below(VM, m);
        if constexpr ((VM >> m) & 1) {
          if constexpr (it + LA >= 16) load_d(std::integral_constant<int, 9 * (it + LA - 16) + n>{});
          else if constexpr (n < 5) dmago<1, (n & 3) * 1024>(vb[n], rws, soB);
          else dmago<0, (n - 5) * 1024>(va[n - 5], rcr, soA);
        }
        if constexpr (it + LA < 16 && ((VM >> (m + 1)) & 1)) {
          constexpr int nn = below(VM, m + 1);
          if constexpr (nn == 0) setm0<((it + LA) & 3) * 20480>(mB);
          if constexpr (nn == 4) setm0<((it + LA) & 3) * 20480 + 4096>(mB);
          if constexpr (nn == 5) setm0<((it + LA) & 3) * 16384>(mA);
        }
        static_assert(LA == 2 && FL1 >= 2 && FL1 + 2 <= 16 - LA && FL1 + 2 <= XS && 4 * (9 * LA + XD * (16 - XS)) <= 16 * XS, "re-tiled weights are read behind their hand-off");
        static_assert((SCS == 0 || (SCS >= FL1 + 2 && (XD == 0 || SCS < XS))) && SX != FL1 && SB2 != FL1 && (SX == 0 || SX > SL) && (SB2 == 0 || SB2 > SL) && (SX != SB2 || SX == 0), "free gaps");
        if constexpr (m == 0 && TA > 0 && it + TA < 16) touch0(tch, vT1, rcr, (it + TA) * 256);
        if constexpr (it == 7 && m < 36) {
          constexpr int k = m / 3;
          if constexpr (m % 3 == 0) vaddi<k * 1024u>(vk4[k], vkr); else if constexpr (m % 3 == 1) vaddi<k * 1024u - 12288u>(vkt, vkr); else vminu(vk4[k], vkt);
        }
        if constexpr (it >= XS && m % 10 == 3 && m / 10 >= 1 && m / 10 <= XD) load_d(std::integral_constant<int, 9 * LA + XD * (it - XS) + m / 10 - 1>{});
        if constexpr (it > 0 && (it == SX || it == SB2 || it == SCS) && m % 10 == 3 && m >= 10) small(std::integral_constant<int, (it == SX ? 0 : it == SCS ? 3 : 6) + m / 10 - 1>{});
        if constexpr (m == 23 && it == FL1) ldv8<1, 0>(flv, vf1, rws, O_FL + 256 * FS0);
        if constexpr ((STG > 0 && m == 13) || (STG > 1 && m == 33) || (STG > 2 && m == 23)) store_rt(std::integral_constant<int, STG * it + (m == 13 ? 0 : m == 33 ? 1 : 2)>{});
        if constexpr (m == 39) {
          if constexpr (it + LA < 15) asm volatile("s_add_u32 %0, %0, 0x100\n\ts_and_b32 %0, %0, 0xf00\n\ts_mul_i32 %1, %0, 0x280" : "+s"(soA), "=s"(soB)::"scc");
        }
        SB;
      });
    });
  });
  sfor<5>([&](auto I_) {
    constexpr int i = I_.value;
    if constexpr (i == 3) { waitl0(); bar(); }
    mf(aP[i], fr[1][1 + i], fr[1][0]);
    mfB<252>(aQ[i], fr[1][1 + i]);
  });
  mfence5(aP);
  mfence5(aQ);
#pragma unroll
  for (int i = 0; i < 5; ++i) {
    const u32x4 o = {pk(aP[i][0], aP[i][1]), pk(aP[i][2], aP[i][3]), pk(aQ[i][0], aQ[i][1]), pk(aQ[i][2], aQ[i][3])};
    st16<SC1>(o, rws, v0 + (i & 3) * 1024, O_PQ + ((4 * rg + w) * 40 + 5 * j + (i & ~3)) * 1024);
  }
  SB;
  auto nbi = [&](int i) { int v = i + odd; v = v >= 7 ? 0 : v; return mi3 >= 12 && i == 6 ? nb0 : nb0 + v; };
  const int k0 = 14 * w - ((w + 1) >> 2);
  const u32 vlast = w < 2 ? v0 : 0x7f000000u;
  u32 vTO = t * 1280 + (TO == 1 ? (q ? 0x7f000000u : chalf) : q == 0 ? chalf : q == 1 ? cfull : q == 2 ? cfull + 64 : 0x7f000000u);
  if constexpr (TO) landed(vTO);
  u32 sW[5];
#pragma unroll
  for (int i = 0; i < 5; ++i) sW[i] = O_W2P + (blk5(j1, i) * 54 + k0) * 1024;
  u32x4 wv[10];
  constexpr int PD = 50 - 9 * LA - XD * (16 - XS), PX = PD + (SX ? 0 : 3), PW = PX + 30, PV = PW + 5, PC = PV + (SCS ? 0 : 3), P2 = PC + 70, PE = P2 + (SB2 ? 0 : 3);
  constexpr int C2 = X21 + X22 + X23, T2 = (C2 + kstail(40) < PW ? kstail(40) : PW - C2), C3 = C2 + T2;
  auto pf = [&](auto N_) {
    constexpr int n = N_.value;
    if constexpr (n < PD) load_d(std::integral_constant<int, 50 - PD + n>{});
    else if constexpr (n < PX) {
      small(std::integral_constant<int, n - PD>{});
    } else if constexpr (n < PV) {
      constexpr int i = (n - PX) / 5, c = (n - PX) % 5;
      if constexpr ((BL & 1) && n == PW) bar();
      const u32 m = l0 + 5120 * w, s = O_W1P + (nbi(i) * 20 + 5 * w) * 1024;
      if constexpr (c == 0 || c == 4 || n == C2 || n == C3) { setm0<i * 20480 + (c == 4 ? 4096 : 0)>(m); brk(); }
      if constexpr (c < 4) dmago<1, c * 1024>(v0, rws, s); else dmago<1, 0>(v0, rws, s + 4096);
    } else if constexpr (n < PC) {
      small(std::integral_constant<int, 3 + n - PV>{});
    } else if constexpr (n < P2) {
      constexpr int c = n - PC, k = c / 5, i = c % 5;
      if constexpr (k < 12) lda<4 * c, 1, 0>(vk4[k], rws, sW[i]);
      else ldv<1, (k & 3) * 1024>(wv[c - 60], k == 12 ? v0 : vlast, rws, sW[i] + 3 * 4096);
    } else if constexpr (n < PE) small(std::integral_constant<int, 6 + n - P2>{});
  };
  static_assert(C2 >= PX && C2 <= PW, "D' and x are requested before the P/Q fragments");
  handoff<0, X21, X22, X23, HB2, 21>(rws, O_FW + gid * FS1 + w * 8, ((lane >> 2) & 1) * FS1 + (lane & 3) * 8, O_FW + (rg1 * 8 + 2 * w) * FS1, tag, pf, [&] {
    if constexpr (TW2) touch(tch, vTW2, rws, 0);
    if constexpr (TO) touch0(tch, vTO, mkrs(p.out, S * D * 2), (u32)(64 * rg1 + 16 * w) * 1280);
  });

  const u32 hb = O_H + (4 * rg1 + w) * 20480;
  {
    f32x4 acc[5];
    float e[20];
    ksplit<10, 40, T2, 20, 33>(acc, wv, vk4, rws, O_PQ + (4 * rg1 * 40 + 10 * w) * 1024, v0, l0, w, lane, [&](auto N_) { pf(std::integral_constant<int, C2 + N_.value>{}); },
      [&](auto C_) { constexpr int c = C_.value; e[c] = upk<c & 1>(c < 8 ? x01[(c >> 1) & 3] : c < 16 ? x23[(c >> 1) & 3] : x4[(c >> 1) & 1]); },
      [&](auto C_, float v) { vadd(e[C_.value], v); });
    u32x2 h[5];
    float s1 = 0.f, s2 = 0.f;
#pragma unroll
    for (int i = 0; i < 5; ++i) {
      h[i][0] = pk(acc[i][0] + e[4 * i], acc[i][1] + e[4 * i + 1]);
      h[i][1] = pk(acc[i][2] + e[4 * i + 2], acc[i][3] + e[4 * i + 3]);
    }
    st16<SC1>(u32x4{h[0][0], h[0][1], h[1][0], h[1][1]}, rws, v0, hb + kp5 * 1024);
    st16<SC1>(u32x4{h[2][0], h[2][1], h[3][0], h[3][1]}, rws, v0 + 1024, hb + kp5 * 1024);
    st8<SC1>(h[4], rws, v0, hb + (blk5(j1, 4) >> 1) * 1024 + (j1 & 1) * 8);
    SB;
#pragma unroll
    for (int i = 0; i < 5; ++i) {
      const float a0 = blo(h[i][0]), a1 = bhi(h[i][0]), a2 = blo(h[i][1]), a3 = bhi(h[i][1]);
      s1 += (a0 + a1) + (a2 + a3);
      s2 += a0 * a0 + a1 * a1 + a2 * a2 + a3 * a3;
    }
    s1 = addx32(addx16(s1));
    s2 = addx32(addx16(s2));
    if (lane < 16) st8<SC1>(u32x2{__float_as_uint(s1), __float_as_uint(s2)}, rws, v0, O_ST + (4 * rg1 + w) * 1024 + (j1 >> 1) * 256 + (j1 & 1) * 8);
    sfor<5>([&](auto I_) { toa<240 + 2 * I_.value>(h[I_.value][0]); toa<241 + 2 * I_.value>(h[I_.value][1]); });
  }
  SB;
  constexpr int C4 = C3 + X31 + X32 + X33;
  static_assert(C3 + X31 + X32 >= PC && C3 + X31 <= PW, "w1 and its column sums are older than the poll; the last block waits for the reduction buffer");
  handoff<C3, X31, X32, X33, HB3 | !(BL & 1), 25>(rws, O_FW + (256 + rg1 * 8 + j1) * FS1 + w * 8, ((lane >> 1) & 7) * FS1 + (lane & 1) * 8, O_FW + (256 + sg3 * 16 + (w >> 1) * 8) * FS1 + (w & 1) * 16, tag, pf, [] {});

  constexpr int PN = PE, C5 = C4 + G3.npf;
  static_assert(C5 <= PC + 60 && G3.nst >= 4, "w2 fragments that stay in vector registers come later");
  {
    u32x4 a[21][2], stv[2];
    auto loadH = [&](auto N_) {
      constexpr int k = N_.value >> 1, m = N_.value & 1;
      ldv<1, (k & 3) * 1024>(a[k][m], v0, rws, O_H + ((8 * sg3 + 2 * w + m) * 20 + (k & ~3)) * 1024);
    };
    sfor<2>([&](auto M_) { ldv<1, 0>(stv[M_.value], v0, rws, O_ST + (8 * sg3 + 2 * w + M_.value) * 1024); brk(); });
    sfor<2 * R3>([&](auto N_) { loadH(N_); brk(); });
    landed(csv); landed(csu); landed(b1v);
    if (tid < 112) {
      const float cs = (__uint_as_float(csv[0]) + __uint_as_float(csv[1])) + (__uint_as_float(csv[2]) + __uint_as_float(csv[3])) + (__uint_as_float(csu[0]) + __uint_as_float(csu[1]) + __uint_as_float(csu[2]));
      const u32 hi = pk(cs, 0.f), hl = pk(cs, cs - blo(hi));
      sts16(l0 + L_CS + tid * 16, u32x4{hl, hi | (b1v << 16), b1v, 0u});
    }
    waitl0();
    bar();
    f32x4 acc[2][7];
    u32x4 wq[6];
    u32 lb0 = l0 + v0, lb1 = lb0 + 61440, lb2 = lb0 + 122880, lbx = l0 + L_CS + t * 16;
    landed(lb1); landed(lb2); landed(lbx);
    auto rdw = [&](auto N_) {
      constexpr int n = N_.value;
      if constexpr (n < 147) {
        constexpr int k = G3.ks[n], i = G3.blk[n], o = (i * 20 + k) * 1024;
        if constexpr (k == 20) dsr<i * 256>(wq[n % 6], lbx);
        else if constexpr (o < 61440) dsr<o>(wq[n % 6], lb0); else if constexpr (o < 122880) dsr<o - 61440>(wq[n % 6], lb1); else dsr<o - 122880>(wq[n % 6], lb2);
      }
    };
    sfor<4>(rdw);
    float sv[2][4];
    u32 xo[2][3];
    auto prep = [&](auto O_) {
      constexpr int m = O_.value & 1, o = O_.value >> 1;
      float(&v)[4] = sv[m];
      u32(&x)[3] = xo[m];
      if constexpr (o < 19) statop<o>(stv[m], v);
      else if constexpr (o == 19) vsqrt(v[1]);
      else if constexpr (o == 20) vkeep16<1>(v[0]);
      else if constexpr (o == 21) vkeep16<0>(v[1]);
      else if constexpr (o == 22) vcvt(x[0], v[0], v[0]);
      else if constexpr (o == 23) vcvt(x[2], v[1], v[1]);
      else if constexpr (o == 24) v[2] = upk<0>(x[0]);
      else if constexpr (o == 25) v[3] = upk<0>(x[2]);
      else if constexpr (o == 26) vrsub(v[2], v[0]);
      else if constexpr (o == 27) vrsub(v[3], v[1]);
      else if constexpr (o == 28) vcvt(x[1], v[2], v[1]);
      else if constexpr (o == 29) vcvt0(x[2], v[3]);
      else if constexpr (o == 30) { a[20][m] = u32x4{x[0], x[1], x[2], 0u}; landed(a[20][m]); }
    };
    static_assert(7 * KX >= 33 && KX <= K1, "the correction operands are ready");
    const int kp = (nb0 + 1) >> 1;
    u32 fo[4][2][4];
    float tq[4];
    auto resop = [&](auto E_) {
      constexpr int e = E_.value, g = e < 64 ? e / 32 : e < 80 ? 2 : 3, r = e - (g < 3 ? 32 * g : 80), m = g == 2 ? r / 8 : r / 16, hb_ = g == 2 ? 0 : (r / 8) & 1;
      constexpr int i = g == 2 ? 6 : (g == 3 ? 4 : 2 * g) + hb_, o = r % 8;
      if constexpr (o < 4) vsqa(tq[o], acc[m][i][o]);
      else if constexpr (o < 6) vcvt(fo[g][m][2 * hb_ + o - 4], tq[2 * (o - 4)], tq[2 * (o - 4) + 1]);
      else vrelu2(fo[g][m][2 * hb_ + o - 6]);
    };
    u32 vf6 = mi3 < 12 ? v0 : 0x7f000000u;
    landed(vf6);
    auto storeF = [&](auto S_) {
      constexpr int g = S_.value >> 1, m = S_.value & 1;
      const u32 fb = O_F + (8 * sg3 + 2 * w + m) * 55296;
      if constexpr (g == 2) sta8(u32x2{fo[g][m][0], fo[g][m][1]}, vf6, rws, fb + (nbi(6) >> 1) * 1024 + odd * 8);
      else sta16<(g == 3 ? 2 : g) * 1024>(u32x4{fo[g][m][0], fo[g][m][1], fo[g][m][2], fo[g][m][3]}, v0, rws, fb + kp * 1024);
    };
    SB;
    sfor<147>([&](auto N_) {
      constexpr int n = N_.value, k = G3.ks[n], i = G3.blk[n], vo = G3.vop[n];
      static_assert(G3.wait[n] >= -1, "h is requested before it is used");
      if constexpr (G3.wait[n] >= 0) waitv<G3.wait[n]>();
      if constexpr (n % 2 == 0) waitl<(n + 4 < 147 ? 2 : n + 2 < 147 ? 145 - n : 0)>();
      if constexpr (k == 0) mf0(acc[0][i], wq[n % 6], a[k][0]); else mf(acc[0][i], wq[n % 6], a[k][0]);
      rdw(std::integral_constant<int, n + 4>{});
      if constexpr (n < 31) prep(std::integral_constant<int, 2 * n>{});
      sfor<G3.e1[n] - G3.e0[n]>([&](auto J_) { resop(std::integral_constant<int, G3.e0[n] + J_.value>{}); });
      SB;
      if constexpr (k == 0) mf0(acc[1][i], wq[n % 6], a[k][1]); else mf(acc[1][i], wq[n % 6], a[k][1]);
      if constexpr (vo >= 200) storeF(std::integral_constant<int, vo - 200>{});
      else if constexpr (vo >= 100) pf(std::integral_constant<int, C4 + vo - 100>{});
      else if constexpr (vo >= 0) loadH(std::integral_constant<int, vo>{});
      if constexpr (n < 31) prep(std::integral_constant<int, 2 * n + 1>{});
      sfor<G3.e0[n + 1] - G3.e1[n]>([&](auto J_) { resop(std::integral_constant<int, G3.e1[n] + J_.value>{}); });
      SB;
    });
    sfor<80 - G3.e0[147]>([&](auto J_) { resop(std::integral_constant<int, G3.e0[147] + J_.value>{}); });
    sfor<6 - G3.nst>([&](auto J_) { storeF(std::integral_constant<int, G3.nst + J_.value>{}); });
    asm volatile("s_nop 7" : "+v"(acc[0][4]), "+v"(acc[0][5]), "+v"(acc[1][4]), "+v"(acc[1][5]));
    sfor<2>([&](auto M_) {
      sfor<16>([&](auto J_) { resop(std::integral_constant<int, 80 + 16 * M_.value + J_.value>{}); });
      storeF(std::integral_constant<int, 6 + M_.value>{});
    });
  }
  constexpr int Y41 = X41 < PN - C5 ? X41 : PN - C5, Y42 = X42 < PN - C5 - Y41 ? X42 : PN - C5 - Y41, Y43 = PN - C5 - Y41 - Y42;
  {
    const int plo = (0xb840 >> (4 * w)) & 15, phi = 4 * w + 3;
    const int pm = plo + (t >> 1) < phi ? plo + (t >> 1) : phi;
    handoff<C5, Y41, Y42, Y43, HB4 | !(BL & 2), 29>(rws, O_FW + (512 + sg3 * 16 + mi3) * FS1 + w * 8, pm * FS1 + (lane & 1) * 8, O_FW + (512 + (rg1 >> 1) * 16) * FS1 + (rg1 & 1) * 16, tag, pf, [] {});
  }

  {
    f32x4 acc[5];
    float e[20], eb[20], sv[4];
    u32 hr[10];
    u32x4 stv;
    ldv<1, 0>(stv, v0, rws, O_ST + (4 * rg1 + w) * 1024);
    brk();
    ksplit<14, 54, 0, 90, 39>(acc, wv, vk4, rws, O_F + (4 * rg1 * 54 + k0) * 1024, vlast, l0, w, lane, [](auto) {},
      [&](auto C_) {
        constexpr int o = C_.value - 20, c = (o - 10) / 3;
        if constexpr (o < -1) statop<o + 20>(stv, sv);
        else if constexpr (o == -1) vrcp(sv[1]);
        else if constexpr (o < 10) hr[o] = froma<240 + o>();
        else if constexpr ((o - 10) % 3 == 0) e[c] = upk<c & 1>(hr[c >> 1]);
        else if constexpr ((o - 10) % 3 == 1) eb[c] = upk<c & 1>(c < 8 ? b01[(c >> 1) & 3] : c < 16 ? b23[(c >> 1) & 3] : b4[(c >> 1) & 1]);
        else vadd(e[c], eb[c]);
      },
      [&](auto C_, float v) { vfma(e[C_.value], v, sv[1]); });
    landed(tch);
    const rsrc_t rout = mkrs(p.out, S * D * 2);
    u32x2 o[5];
#pragma unroll
    for (int i = 0; i < 5; ++i) {
      o[i][0] = pk(acc[i][0] * sv[1] + e[4 * i], acc[i][1] * sv[1] + e[4 * i + 1]);
      o[i][1] = pk(acc[i][2] * sv[1] + e[4 * i + 2], acc[i][3] * sv[1] + e[4 * i + 3]);
    }
    const u32 vo = vx, so = sx;
    st16<OUT_AUX>(u32x4{o[0][0], o[0][1], o[1][0], o[1][1]}, rout, vo, so + cfull);
    st16<OUT_AUX>(u32x4{o[2][0], o[2][1], o[3][0], o[3][1]}, rout, vo + 64, so + cfull);
    st8<OUT_AUX>(o[4], rout, vo, so + chalf);
  }
  if constexpr (CPF && !CEND) asm volatile(".Lfnet_end:");
}

extern "C" void kernel_launch(void* const* d_in, const int* in_sizes, int n_in, void* d_out, int out_size, void* d_ws, size_t ws_size,
                              hipStream_t stream) {
  Params p;
  p.x = d_in[0]; p.cr = d_in[1]; p.ci = d_in[2]; p.dr = d_in[3]; p.di = d_in[4];
  p.w1 = d_in[6]; p.b1 = d_in[7]; p.w2 = d_in[8]; p.b2 = d_in[9];
  p.out = d_out; p.ws = d_ws;
  (void)hipFuncSetAttribute((const void*)fnet_block, hipFuncAttributeMaxDynamicSharedMemorySize, LDS_BYTES);
  fnet_block<<<256, 256, LDS_BYTES, stream>>>(p);
}
```
